```python
import torch

def _softmax(x, axis=-1):
    return torch.softmax(x, dim=axis)

def _gelu(x):
    return torch.nn.functional.gelu(x, approximate='tanh')

def _silu(x):
    return torch.nn.functional.silu(x)

def _sigmoid(x):
    return torch.sigmoid(x)

def _layernorm(x, axis=-1, eps=1e-05):
    m = x.mean(dim=axis, keepdim=True)
    v = x.var(dim=axis, keepdim=True, unbiased=False)
    return (x - m) / torch.sqrt(v + eps)

def _scatter_add(tgt, idx, src, axis=0):
    return tgt.clone().index_add_(axis, idx, src)

def _segment_sum(src, seg, n):
    out = torch.zeros((n,) + src.shape[1:], dtype=src.dtype)
    return out.index_add_(0, seg, src)

def _pad(x, pw):
    flat = tuple((v for pair in reversed(pw) for v in pair))
    return torch.nn.functional.pad(x, flat)
kernel_name = 'adaLN_cond__sliding_block_cached__dense_activation_squared_relu__decode'

def setup_inputs():

    _w = lambda a: (lambda t: t.to(torch.bfloat16) if t.is_floating_point() else t)(torch.as_tensor(a))

    def _A_setup():
        B, Sq, D = (4, 1, 512)
        Hq, Hkv, Sk, Dh = (32, 8, 8192, 128)
        assert Hq % Hkv == 0, (Hq, Hkv)
        return {'x': _w((lambda *s: torch.randn(*s, dtype=torch.bfloat16) * 0.05)(B, Sq, D)), 'k_cache': _w((lambda *s: torch.randn(*s, dtype=torch.bfloat16) * 3.2)(B, Hkv, Sk, Dh)), 'v_cache': _w((lambda *s: torch.randn(*s, dtype=torch.bfloat16) * 0.4)(B, Hkv, Sk, Dh)), 'wq': _w((lambda *s: torch.randn(*s, dtype=torch.bfloat16) * 3.2)(D, Hq * Dh) * D ** (-0.5)), 'wo': _w((lambda *s: torch.randn(*s, dtype=torch.bfloat16) * 0.4)(Hq * Dh, D) * (Hq * Dh) ** (-0.5))}
    _A_in = _A_setup()

    def _B_setup():
        B, S, D, F = (4, 1, 512, 1792)
        return {'x': _w((lambda *s: torch.randn(*s, dtype=torch.bfloat16) * 0.05)(B, S, D)), 'w1': _w((lambda *s: torch.randn(*s, dtype=torch.bfloat16) * 0.05)(D, F)), 'b1': _w((lambda *s: torch.randn(*s, dtype=torch.bfloat16) * 0.4)(F)), 'w2': _w((lambda *s: torch.randn(*s, dtype=torch.bfloat16) * 0.05)(F, D)), 'b2': _w((lambda *s: torch.randn(*s, dtype=torch.bfloat16) * 3.2)(D))}
    _B_in = _B_setup()
    return {'A_x': _A_in['x'], 'A_k_cache': _A_in['k_cache'], 'A_v_cache': _A_in['v_cache'], 'A_wq': _A_in['wq'], 'A_wo': _A_in['wo'], 'B_x': _B_in['x'], 'B_w1': _B_in['w1'], 'B_b1': _B_in['b1'], 'B_w2': _B_in['w2'], 'B_b2': _B_in['b2'], 'c': _w((lambda *s: torch.randn(*s, dtype=torch.bfloat16) * 0.1)(4, 512) * 8.0), 'w_mod': _w((lambda *s: torch.randn(*s, dtype=torch.bfloat16) * 0.1)(512, 6 * 512) * 512 ** (-0.5) * 8.0)}

def reference(A_x, A_k_cache, A_v_cache, A_wq, A_wo, B_x, B_w1, B_b1, B_w2, B_b2, c, w_mod):

    def _A_ref(x, k_cache, v_cache, wq, wo):
        B, Sq, D = x.shape
        Hq, Dh = (32, 128)
        Hkv, Sk = (k_cache.shape[1], k_cache.shape[2])
        W = min(4096, max(256, Sk // 4))
        rep = Hq // Hkv
        q = (lambda x, *d: x.permute(*d))((x @ wq).reshape(B, Sq, Hq, Dh), 0, 2, 1, 3)
        k = (lambda x, n, axis: torch.repeat_interleave(x, n, dim=axis))(k_cache[:, :, Sk - W:, :], rep, axis=1)
        v = (lambda x, n, axis: torch.repeat_interleave(x, n, dim=axis))(v_cache[:, :, Sk - W:, :], rep, axis=1)
        scores = torch.einsum('bhqd,bhkd->bhqk', q, k) * Dh ** (-0.5)
        p = _softmax(scores, axis=-1)
        o = torch.einsum('bhqk,bhkd->bhqd', p, v)
        return (lambda x, *d: x.permute(*d))(o, 0, 2, 1, 3).reshape(B, Sq, Hq * Dh) @ wo

    def _B_ref(x, w1, b1, w2, b2):
        h = torch.einsum('bsd,df->bsf', x, w1) + b1
        zero = torch.zeros_like(h)
        r = torch.where(h > zero, h, zero)
        h = r * r
        out = torch.einsum('bsf,fd->bsd', h, w2) + b2
        return out
    x = A_x
    mod = (_silu(c) @ w_mod).reshape(c.shape[0], 6, 1, -1)
    sa, ca, ga, sb, cb, gb = (mod[:, 0].clone(), mod[:, 1].clone(), mod[:, 2].clone(), mod[:, 3].clone(), mod[:, 4].clone(), mod[:, 5].clone())
    h = _layernorm(x, axis=-1) * (1 + ca) + sa
    x = x + ga * _A_ref(h, A_k_cache, A_v_cache, A_wq, A_wo)
    h = _layernorm(x, axis=-1) * (1 + cb) + sb
    x = x + gb * _B_ref(h, B_w1, B_b1, B_w2, B_b2)
    return x
```

```cpp
#include <hip/hip_runtime.h>
#include <hip/hip_bf16.h>
#include <stdint.h>

#define MIMIC 1

typedef unsigned short u16;
__device__ __forceinline__ float bf2f(u16 b) { return __uint_as_float(((unsigned)b) << 16); }
__device__ __forceinline__ float rbf(float f) {
  unsigned u = __float_as_uint(f); u += 0x7FFFu + ((u >> 16) & 1u); return __uint_as_float(u & 0xFFFF0000u);
}
__device__ __forceinline__ u16 f2bf(float f) { unsigned u = __float_as_uint(f); u += 0x7FFFu + ((u >> 16) & 1u); return (u16)(u >> 16); }

constexpr int B = 4, D = 512, HQ = 32, HKV = 8, SK = 8192, DH = 128, W = 2048, F = 1792;

__device__ float block_sum(float v, float* red) {
  for (int o = 32; o > 0; o >>= 1) v += __shfl_xor(v, o);
  __syncthreads();
  if ((threadIdx.x & 63) == 0) red[threadIdx.x >> 6] = v;
  __syncthreads();
  float s = 0; for (int i = 0; i < (int)(blockDim.x >> 6); ++i) s += red[i];
  return s;
}

__global__ void k_mod(const u16* c, const u16* wmod, float* mod) {
  __shared__ float sc[B][D]; __shared__ float red[4][B][64];
  for (int i = threadIdx.x; i < B * D; i += 256) { float v = bf2f(c[i]); sc[i / D][i % D] = rbf(v / (1.f + __expf(-v))); }
  __syncthreads();
  int col = blockIdx.x * 64 + (threadIdx.x & 63), ks = threadIdx.x >> 6;
  float a[B] = {0, 0, 0, 0};
  for (int k = ks * 128; k < ks * 128 + 128; ++k) { float w = bf2f(wmod[k * 3072 + col]); for (int b = 0; b < B; ++b) a[b] += sc[b][k] * w; }
  for (int b = 0; b < B; ++b) red[ks][b][threadIdx.x & 63] = a[b];
  __syncthreads();
  int b = threadIdx.x >> 6, cc = threadIdx.x & 63;
  mod[b * 3072 + blockIdx.x * 64 + cc] = rbf(red[0][b][cc] + red[1][b][cc] + red[2][b][cc] + red[3][b][cc]);
}

__device__ void ln_mod(const float* xrow  , const float* shift, const float* scale, float* hout, float* red) {
  float v0 = xrow[threadIdx.x], v1 = xrow[threadIdx.x + 256];
  float mean32 = block_sum(v0 + v1, red) / D;
  float var32 = block_sum((v0 - mean32) * (v0 - mean32) + (v1 - mean32) * (v1 - mean32), red) / D;
  float m = rbf(mean32), var = rbf(var32);
  float sd = rbf(sqrtf(rbf(var + 1e-5f)));
  for (int t = 0; t < 2; ++t) { int d = threadIdx.x + t * 256; float v = t ? v1 : v0;
    float n = rbf(rbf(v - m) / sd);
    hout[d] = rbf(rbf(n * rbf(1.f + scale[d])) + shift[d]); }
  __syncthreads();
}

__global__ void k_q(const u16* x, const float* mod, const u16* wq, float* q) {
  __shared__ float xs[D]; __shared__ float h[B][D]; __shared__ float red[4][B][64]; __shared__ float r2[8];
  for (int b = 0; b < B; ++b) {
    for (int i = threadIdx.x; i < D; i += 256) xs[i] = bf2f(x[b * D + i]);
    __syncthreads();
    ln_mod(xs, mod + b * 3072, mod + b * 3072 + 512, h[b], r2);
  }
  int col = blockIdx.x * 64 + (threadIdx.x & 63), ks = threadIdx.x >> 6;
  float a[B] = {0, 0, 0, 0};
  for (int k = ks * 128; k < ks * 128 + 128; ++k) { float w = bf2f(wq[k * 4096 + col]); for (int b = 0; b < B; ++b) a[b] += h[b][k] * w; }
  for (int b = 0; b < B; ++b) red[ks][b][threadIdx.x & 63] = a[b];
  __syncthreads();
  int b = threadIdx.x >> 6, cc = threadIdx.x & 63;
  q[b * 4096 + blockIdx.x * 64 + cc] = rbf(red[0][b][cc] + red[1][b][cc] + red[2][b][cc] + red[3][b][cc]);
}

__global__ void __launch_bounds__(256) k_attn(const float* q, const u16* kc, const u16* vc, float* o) {
  __shared__ float qs[DH]; __shared__ float s[W]; __shared__ float red[8]; __shared__ float part[2][DH];
  int b = blockIdx.x / HQ, h = blockIdx.x % HQ, g = h / 4;
  if (threadIdx.x < DH) qs[threadIdx.x] = q[b * 4096 + h * DH + threadIdx.x];
  __syncthreads();
  const u16* kb = kc + ((size_t)(b * HKV + g) * SK + (SK - W)) * DH;
  const u16* vb = vc + ((size_t)(b * HKV + g) * SK + (SK - W)) * DH;
  float mx = -INFINITY;
  for (int k = threadIdx.x; k < W; k += 256) {
    float a = 0; const uint4* kr = (const uint4*)(kb + (size_t)k * DH);
    #pragma unroll 2
    for (int j = 0; j < 16; ++j) { uint4 u = kr[j]; unsigned w[4] = {u.x, u.y, u.z, u.w};
      #pragma unroll
      for (int t = 0; t < 4; ++t) { a += qs[j * 8 + t * 2] * __uint_as_float(w[t] << 16); a += qs[j * 8 + t * 2 + 1] * __uint_as_float(w[t] & 0xFFFF0000u); } }
    a = rbf(rbf(a) * 0.08838834764831845f);
    s[k] = a; mx = fmaxf(mx, a);
  }
  for (int o2 = 32; o2 > 0; o2 >>= 1) mx = fmaxf(mx, __shfl_xor(mx, o2));
  __syncthreads();
  if ((threadIdx.x & 63) == 0) red[threadIdx.x >> 6] = mx;
  __syncthreads();
  mx = fmaxf(fmaxf(red[0], red[1]), fmaxf(red[2], red[3]));
  float sum = 0;
  for (int k = threadIdx.x; k < W; k += 256) { float e = __expf(s[k] - mx); s[k] = e; sum += e; }
  sum = block_sum(sum, red);
  for (int k = threadIdx.x; k < W; k += 256) s[k] = rbf(s[k] / sum);
  __syncthreads();
  int d = threadIdx.x & 127, half = threadIdx.x >> 7;
  float a = 0;
  for (int k = half * 1024; k < half * 1024 + 1024; ++k) a += s[k] * bf2f(vb[(size_t)k * DH + d]);
  part[half][d] = a;
  __syncthreads();
  if (threadIdx.x < DH) o[b * 4096 + h * DH + threadIdx.x] = rbf(part[0][threadIdx.x] + part[1][threadIdx.x]);
}

__global__ void k_wo(const u16* x, const float* mod, const float* o, const u16* wo, float* x1) {
  __shared__ float red[16][B][16];
  int cc = threadIdx.x & 15, ks = threadIdx.x >> 4, col = blockIdx.x * 16 + cc;
  float a[B] = {0, 0, 0, 0};
  for (int k = ks * 256; k < ks * 256 + 256; ++k) { float w = bf2f(wo[k * 512 + col]); for (int b = 0; b < B; ++b) a[b] += o[b * 4096 + k] * w; }
  for (int b = 0; b < B; ++b) red[ks][b][cc] = a[b];
  __syncthreads();
  if (threadIdx.x < 64) { int b = threadIdx.x >> 4; float s = 0; for (int i = 0; i < 16; ++i) s += red[i][b][cc];
    x1[b * D + col] = rbf(bf2f(x[b * D + col]) + rbf(mod[b * 3072 + 1024 + col] * rbf(s))); }
}

__global__ void k_w1(const float* x1, const float* mod, const u16* w1, const u16* b1, float* h1) {
  __shared__ float h[B][D]; __shared__ float red[4][B][64]; __shared__ float r2[8];
  for (int b = 0; b < B; ++b) ln_mod(x1 + b * D, mod + b * 3072 + 1536, mod + b * 3072 + 2048, h[b], r2);
  int col = blockIdx.x * 64 + (threadIdx.x & 63), ks = threadIdx.x >> 6;
  float a[B] = {0, 0, 0, 0};
  for (int k = ks * 128; k < ks * 128 + 128; ++k) { float w = bf2f(w1[k * F + col]); for (int b = 0; b < B; ++b) a[b] += h[b][k] * w; }
  for (int b = 0; b < B; ++b) red[ks][b][threadIdx.x & 63] = a[b];
  __syncthreads();
  int b = threadIdx.x >> 6, cc = threadIdx.x & 63;
  float v = rbf(rbf(red[0][b][cc] + red[1][b][cc] + red[2][b][cc] + red[3][b][cc]) + bf2f(b1[blockIdx.x * 64 + cc]));
  v = v > 0.f ? v : 0.f;
  h1[b * F + blockIdx.x * 64 + cc] = rbf(v * v);
}

__global__ void k_w2(const float* x1, const float* mod, const float* h1, const u16* w2, const u16* b2, u16* out) {
  __shared__ float red[16][B][16];
  int cc = threadIdx.x & 15, ks = threadIdx.x >> 4, col = blockIdx.x * 16 + cc;
  float a[B] = {0, 0, 0, 0};
  for (int k = ks * 112; k < ks * 112 + 112; ++k) { float w = bf2f(w2[k * 512 + col]); for (int b = 0; b < B; ++b) a[b] += h1[b * F + k] * w; }
  for (int b = 0; b < B; ++b) red[ks][b][cc] = a[b];
  __syncthreads();
  if (threadIdx.x < 64) { int b = threadIdx.x >> 4; float s = 0; for (int i = 0; i < 16; ++i) s += red[i][b][cc];
    float y = rbf(rbf(s) + bf2f(b2[col]));
    out[b * D + col] = f2bf(x1[b * D + col] + rbf(mod[b * 3072 + 2560 + col] * y)); }
}

extern "C" void kernel_launch(void* const* d_in, const int* in_sizes, int n_in, void* d_out, int out_size, void* d_ws, size_t ws_size, hipStream_t stream) {
  const u16* x = (const u16*)d_in[0]; const u16* kc = (const u16*)d_in[1]; const u16* vc = (const u16*)d_in[2];
  const u16* wq = (const u16*)d_in[3]; const u16* wo = (const u16*)d_in[4];
  const u16* w1 = (const u16*)d_in[6]; const u16* b1 = (const u16*)d_in[7]; const u16* w2 = (const u16*)d_in[8]; const u16* b2 = (const u16*)d_in[9];
  const u16* c = (const u16*)d_in[10]; const u16* wmod = (const u16*)d_in[11];
  float* ws = (float*)d_ws;
  float* mod = ws; float* q = ws + 16384; float* o = ws + 32768; float* x1 = ws + 49152; float* h1 = ws + 53248;
  k_mod<<<48, 256, 0, stream>>>(c, wmod, mod);
  k_q<<<64, 256, 0, stream>>>(x, mod, wq, q);
  k_attn<<<128, 256, 0, stream>>>(q, kc, vc, o);
  k_wo<<<32, 256, 0, stream>>>(x, mod, o, wo, x1);
  k_w1<<<28, 256, 0, stream>>>(x1, mod, w1, b1, h1);
  k_w2<<<32, 256, 0, stream>>>(x1, mod, h1, w2, b2, (u16*)d_out);
}
```

```cpp
#include <hip/hip_runtime.h>
#include <stdint.h>

typedef unsigned short u16;
typedef unsigned u32;
typedef u32 u32x2 __attribute__((ext_vector_type(2)));
typedef u32 u32x4 __attribute__((ext_vector_type(4)));
typedef float f32x2 __attribute__((ext_vector_type(2)));
typedef float f32x4 __attribute__((ext_vector_type(4)));
typedef __bf16 bf16x2 __attribute__((ext_vector_type(2)));
typedef __bf16 bf16x8 __attribute__((ext_vector_type(8)));
typedef short s16x4 __attribute__((ext_vector_type(4)));
typedef __amdgpu_buffer_rsrc_t rsrc_t;
#define LAS __attribute__((address_space(3)))
#define GAS __attribute__((address_space(1)))
#define DI __device__ __forceinline__

#define K_NAP 16
#define CODE_KB 17
constexpr u32 T_QPOLL = 370;
constexpr u32 T_WO = 575, D_SLOW = 50;
constexpr u32 T_RED = 690;
constexpr u32 D_HU = 88;
constexpr u32 D_S6 = 99;
constexpr u32 D_S6B = 84;

constexpr u32 WS_EPOCH = 0;
constexpr u32 WS_FLAG = 1024;
constexpr u32 WS_S1 = 4096;
constexpr u32 WS_S2 = WS_S1 + 16384;
constexpr u32 WS_S3 = WS_S2 + 65536;
constexpr u32 S4_SLOT = 1152;
constexpr u32 WS_S4 = WS_S3 + 16384;
constexpr u32 WS_S5 = WS_S4 + 128 * S4_SLOT;
constexpr u32 WS_S6 = WS_S5 + 4 * 32768;
constexpr u32 WS_HU = WS_S6 + 64 * 7168;
constexpr u32 WS_BYTES = WS_HU + 8192;
constexpr u32 CLR = (WS_BYTES - WS_S3) / 64;
static_assert(CLR % 16 == 0 && CLR > 8192 && CLR <= 16384, "S3..HU = 64 shares of two stores per thread");

constexpr u32 L_WAVE = 16384;
constexpr u32 L_Q = 8 * L_WAVE;
constexpr u32 L_ML = L_Q + 1024;
constexpr u32 L_ST = L_ML + 256;
constexpr u32 L_FLAG = L_ST + 256;
constexpr u32 L_RED = L_FLAG + 256;
constexpr u32 L_BYTES = L_RED + 8192;

constexpr u32 SENT = 0xFFFFFFFFu;
constexpr int AUX_SC1 = 16;
constexpr u32 SPIN_CAP = 1u << 16;

DI rsrc_t mk_rsrc(const void* p, u32 bytes) { return __builtin_amdgcn_make_buffer_rsrc((void*)p, 0, (int)bytes, 0x00020000); }
DI u32 now() { return (u32)__builtin_amdgcn_s_memrealtime(); }
DI u32 cvtpk(float lo, float hi) { f32x2 v = {lo, hi}; return __builtin_bit_cast(u32, __builtin_convertvector(v, bf16x2)); }
DI float bflo(u32 p) { return __uint_as_float(p << 16); }
DI float bfhi(u32 p) { return __uint_as_float(p & 0xFFFF0000u); }
DI float rbf(float a) { return bflo(cvtpk(a, a)); }
DI u32 nosent(u32 v) { return v < 0xFFFFFFFEu ? v : 0xFFFFFFFEu; }
template <int CTRL> DI u32 dppu(u32 v) { return (u32)__builtin_amdgcn_mov_dpp((int)v, CTRL, 0xf, 0xf, true); }
template <int CTRL> DI float dppf(float v) { return __uint_as_float(dppu<CTRL>(__float_as_uint(v))); }
constexpr int QP(int a, int b, int c, int d) { return a | (b << 2) | (c << 4) | (d << 6); }
constexpr int XOR1 = QP(1, 0, 3, 2), XOR2 = QP(2, 3, 0, 1), HMIR = 0x141;
DI float rs32(float a, float b) { auto t = __builtin_amdgcn_permlane32_swap(__float_as_uint(a), __float_as_uint(b), false, false); return __uint_as_float(t[0]) + __uint_as_float(t[1]); }
DI float rs16(float a, float b) { auto t = __builtin_amdgcn_permlane16_swap(__float_as_uint(a), __float_as_uint(b), false, false); return __uint_as_float(t[0]) + __uint_as_float(t[1]); }
DI float vmax(float a, float b) { return __builtin_amdgcn_fmed3f(a, b, __builtin_inff()); }
DI float mx32(float a) { auto t = __builtin_amdgcn_permlane32_swap(__float_as_uint(a), __float_as_uint(a), false, false); return vmax(__uint_as_float(t[0]), __uint_as_float(t[1])); }
DI float mx16(float a) { auto t = __builtin_amdgcn_permlane16_swap(__float_as_uint(a), __float_as_uint(a), false, false); return vmax(__uint_as_float(t[0]), __uint_as_float(t[1])); }
DI f32x4 mfma(u32x4 a, u32x4 b, f32x4 c) { return __builtin_amdgcn_mfma_f32_16x16x32_bf16(__builtin_bit_cast(bf16x8, a), __builtin_bit_cast(bf16x8, b), c, 0, 0, 0); }
template <int NT> DI void dma(rsrc_t rs, u32 voff, u32 soff, u32 lds) {
  if (NT) asm volatile("s_mov_b32 m0, %0\n\ts_nop 0\n\tbuffer_load_dwordx4 %1, %2, %3 offen nt lds" ::"s"(lds), "v"(voff), "s"(rs), "s"(soff) : "memory");
  else asm volatile("s_mov_b32 m0, %0\n\ts_nop 0\n\tbuffer_load_dwordx4 %1, %2, %3 offen lds" ::"s"(lds), "v"(voff), "s"(rs), "s"(soff) : "memory");
}
#define WAIT_VM(n) asm volatile("s_waitcnt vmcnt(" #n ")" ::: "memory")
#define BARRIER() do { asm volatile("s_waitcnt lgkmcnt(0)" ::: "memory"); __builtin_amdgcn_s_barrier(); asm volatile("" ::: "memory"); } while (0)
#define OPAQUE() asm volatile("" ::: "memory")
DI u32x4 ld128(rsrc_t rs, u32 off) { return __builtin_amdgcn_raw_buffer_load_b128(rs, off, 0, AUX_SC1); }
DI u32x2 ld64(rsrc_t rs, u32 off) { return __builtin_amdgcn_raw_buffer_load_b64(rs, off, 0, AUX_SC1); }
DI u32 ld32(rsrc_t rs, u32 off) { return __builtin_amdgcn_raw_buffer_load_b32(rs, off, 0, AUX_SC1); }
DI void st128(rsrc_t rs, u32 off, u32x4 v) { __builtin_amdgcn_raw_buffer_store_b128(v, rs, off, 0, AUX_SC1); }
DI void st64(rsrc_t rs, u32 off, u32x2 v) { __builtin_amdgcn_raw_buffer_store_b64(v, rs, off, 0, AUX_SC1); }
DI u32 umax(u32 a, u32 b) { return a > b ? a : b; }
DI bool none(bool bad) { return __builtin_amdgcn_ballot_w64(bad) == 0; }
#define SB() __builtin_amdgcn_sched_barrier(0)
#define PIN(...) asm volatile("" : __VA_ARGS__)
DI u32x2 tr_read(LAS unsigned char* L, u32 addr) { return __builtin_bit_cast(u32x2, __builtin_amdgcn_ds_read_tr16_b64_v4i16((LAS s16x4*)(L + addr))); }
DI u32x4 tr_frag(LAS unsigned char* L, u32 a0, u32 a1) { u32x2 lo = tr_read(L, a0), hi = tr_read(L, a1); return u32x4{lo.x, lo.y, hi.x, hi.y}; }
DI void wait_until(u32 t0, u32 ticks) { while (now() - t0 < ticks) __builtin_amdgcn_s_sleep(1); }

DI u32x4 pair_frag(u32 d0, u32 d1) { return u32x4{dppu<QP(0, 0, 2, 2)>(d0), dppu<QP(0, 0, 2, 2)>(d1), dppu<QP(1, 1, 3, 3)>(d0), dppu<QP(1, 1, 3, 3)>(d1)}; }


#define TAKE_PROLOGUE(N) \
  u32 tag, av0, av1; \
  asm volatile("s_waitcnt vmcnt(" #N ")\n\tv_add_u32 %0, %3, 1\n\tv_mov_b32 %1, %4\n\tv_mov_b32 %2, %5" \
               : "=&v"(tag), "=&v"(av0), "=&v"(av1) : "s"(ep), "v"(av.x), "v"(av.y), "v"(cv) : "memory")
constexpr float LOG2E = 1.4426950408889634f;
#define POLL_BEGIN for (u32 spin_ = 0;;) { OPAQUE();
#define POLL_END(bad, napstmt) if (__builtin_expect(none(bad), 1)) break; napstmt; if (++spin_ > SPIN_CAP) break; }
DI void wait_flags(rsrc_t ws, u32 lane, u32 tag) {
  POLL_BEGIN
    const u32x2 f = ld64(ws, WS_FLAG + lane * 8);
  POLL_END((f.x != tag) | (f.y != ~tag), __builtin_amdgcn_s_sleep(4))
}
DI u32x4 pair_selector(u32 idx) {
  const u32 one = (idx & 1) ? 0x3F800000u : 0x00003F80u, dq = idx >> 2;
  return u32x4{dq == 0 ? one : 0u, dq == 1 ? one : 0u, dq == 2 ? one : 0u, dq == 3 ? one : 0u};
}
DI u32 wait_heads_up(rsrc_t ws, u32 x, u32 lane, u32 t0) {
  u32 hu;
  POLL_BEGIN
    hu = ld32(ws, WS_HU + x * 1024 + (lane & 7) * 128);
  POLL_END(hu == SENT, __builtin_amdgcn_s_sleep(1))
  int d = (int)(hu - t0);
  d = max(d, (int)dppu<XOR1>(d)); d = max(d, (int)dppu<XOR2>(d)); d = max(d, (int)dppu<HMIR>(d));
  const u32 tl = t0 + __builtin_amdgcn_readfirstlane(d);
  while (now() - tl < D_HU) {}
  return tl + D_HU;
}
DI u32x4 quad_frag(u32 d) { return u32x4{dppu<QP(0, 0, 0, 0)>(d), dppu<QP(1, 1, 1, 1)>(d), dppu<QP(2, 2, 2, 2)>(d), dppu<QP(3, 3, 3, 3)>(d)}; }
DI f32x2 unpk(u32 d) { return f32x2{bflo(d), bfhi(d)}; }

#define STR2(x) #x
#define STR(x) STR2(x)
#define DMA_W(v_, s_) "s_addk_i32 m0, 0x400\n\ts_add_u32 s55, s55, " s_ "\n\tbuffer_load_dwordx4 " v_ ", s[44:47], s55 offen lds\n\t"
#define DMA_K(v_, s_) "buffer_load_dwordx4 " v_ ", s[36:39], " s_ " offen nt lds\n\ts_addk_i32 m0, 0x400\n\ts_addk_i32 " s_ ", 0x400\n\ts_and_b32 " s_ ", " s_ ", s56\n\t"

#define MFMA "v_mfma_f32_16x16x32_bf16 "
#define DPPQ(d_, s_, n_) "v_mov_b32_dpp " d_ ", " s_ " quad_perm:[" n_ "," n_ "," n_ "," n_ "] row_mask:0xf bank_mask:0xf\n\t"
#define TMP_CLOBBERS "v128", "v129", "v130", "v131", "v132", "v133", "v134", "v135", "v136", "v137", "v138", "v139", "v140", "v141", \
        "v142", "v143", "v144", "v145", "v146", "v147", "v148", "v149", "v150", "v151", "v152", "v153", "v154", "v155", "v156", "v157", \
        "v158", "v159", "v160", "v161", "v162", "v163", "v164", "v165", "v166", "v167", "v168", "v169", "v170", "v171", "v172", "v173", \
        "v174", "v175", "v176", "v177", "v178", "v179", "v180", "v181", "v182", "v183"

__global__ void __launch_bounds__(512, 2)
fused_decode(const u16* pk, u32 nk_, u32 fk_, const u16* pwm, u32 nm_, u32 fm_, const u16* pwq, u32 nq_, u32 fq_, const u16* pc,
             const u16* px, const u16* pv, const u16* pwo, const u16* pw1, const u16* pb1, const u16* pw2, const u16* pb2,
             u16* pout, unsigned char* pws) {
  __shared__ __attribute__((aligned(1024))) unsigned char lds_[L_BYTES];
  LAS unsigned char* L = (LAS unsigned char*)lds_;
  const u32 lbase = (u32)(uintptr_t)L;
  u32 wg = blockIdx.x, tid = threadIdx.x;
  u32x2 av; u32 cv, v0_, v1_, v2_, v3_, v4_;
  asm volatile(
      "s_load_dwordx16 s[36:51], %[ka], 0x0\n\t"
      "v_readfirstlane_b32 s52, %[tid]\n\t"
      "s_lshr_b32 s53, %[wg], 3\n\t"
      "s_and_b32 s54, %[wg], 7\n\t"
      "v_and_b32 %[v0], 63, %[tid]\n\t"
      "s_lshr_b32 s52, s52, 6\n\t"
      "s_cmp_lt_u32 s53, " STR(CODE_KB) "\n\t"
      "s_cselect_b32 s55, s52, 0\n\t"
      "s_cmp_lg_u32 s55, 7\n\t"
      "s_cbranch_scc1 .Lnocode%=\n\t"
      "s_getpc_b64 s[56:57]\n\t"
      "s_lshl_b32 s55, s53, 10\n\t"
      "v_and_b32 %[v1], 7, %[tid]\n\t"
      "v_lshlrev_b32 %[v1], 7, %[v1]\n\t"
      "v_add_u32 %[v1], s55, %[v1]\n\t"
      "global_load_dword %[cv], %[v1], s[56:57] offset:512\n"
      ".Lnocode%=:\n\t"
      "v_lshrrev_b32 %[v1], 4, %[v0]\n\t"
      "s_cmp_lt_u32 s53, 16\n\t"
      "s_cbranch_scc1 .Latt%=\n\t"
      "s_lshl_b32 s55, s52, 14\n\t"
      "s_add_u32 m0, s55, %[lb]\n\t"
      "v_bfe_u32 %[v2], %[v0], 2, 2\n\t"
      "v_and_b32 %[v3], 3, %[v0]\n\t"
      "s_cmp_lt_u32 s53, 24\n\t"
      "s_cbranch_scc0 .Lwb%=\n\t"
      "s_lshl_b32 s55, s53, 1\n\t"
      "s_add_u32 s55, s55, s52\n\t"
      "s_and_b32 s55, s55, 7\n\t"
      "v_lshlrev_b32 %[v2], 10, %[v2]\n\t"
      "v_lshl_or_b32 %[v2], %[v3], 5, %[v2]\n\t"
      "v_lshl_or_b32 %[v2], %[v1], 3, %[v2]\n\t"
      "s_lshl_b32 s56, s55, 7\n\t"
      "v_or_b32 %[v2], s56, %[v2]\n\t"
      "s_mul_i32 s55, s55, 0x60000\n\t"
      "s_lshl_b32 s56, s54, 3\n\t"
      "s_and_b32 s57, s53, 7\n\t"
      "s_add_u32 s56, s56, s57\n\t"
      "s_lshl_b32 s56, s56, 5\n\t"
      "s_add_u32 s55, s55, s56\n\t"
      "v_lshrrev_b32 %[v3], 1, %[v0]\n\t"
      "v_mul_u32_u24 %[v3], 0x1800, %[v3]\n\t"
      "v_and_b32 %[v4], 1, %[v0]\n\t"
      "v_lshl_add_u32 %[v3], %[v4], 4, %[v3]\n\t"
      "s_waitcnt lgkmcnt(0)\n\t"
      "global_load_dwordx2 %[av], %[v2], s[48:49]\n\t"
      "s_memrealtime s[58:59]\n\t"
      "buffer_load_dwordx4 %[v3], s[40:43], s55 offen lds\n\t"
      "s_addk_i32 m0, 0x400\n\t"
      "s_add_u32 s55, s55, 0x30000\n\t"
      "buffer_load_dwordx4 %[v3], s[40:43], s55 offen lds\n\t"
      "s_branch .Lend%=\n"
      ".Lwb%=:\n\t"
      "v_lshlrev_b32 %[v2], 10, %[v2]\n\t"
      "v_lshl_or_b32 %[v2], %[v3], 3, %[v2]\n\t"
      "v_lshl_or_b32 %[v2], %[v1], 5, %[v2]\n\t"
      "s_lshl_b32 s56, s52, 7\n\t"
      "v_or_b32 %[v2], s56, %[v2]\n\t"
      "s_lshl_b32 s55, s52, 19\n\t"
      "s_lshl_b32 s56, s54, 3\n\t"
      "s_and_b32 s57, s53, 7\n\t"
      "s_add_u32 s56, s56, s57\n\t"
      "s_lshl_b32 s56, s56, 7\n\t"
      "s_add_u32 s55, s55, s56\n\t"
      "s_movk_i32 s56, 0x2000\n\t"
      "s_mov_b32 s57, 0x1e000\n\t"
      "v_and_b32 %[v3], 7, %[v0]\n\t"
      "v_lshrrev_b32 %[v4], 3, %[v0]\n\t"
      "v_lshlrev_b32 %[v4], 14, %[v4]\n\t"
      "v_lshl_or_b32 %[v3], %[v3], 4, %[v4]\n\t"
      "v_xor_b32 %[v4], 32, %[v3]\n\t"
      "v_xor_b32 %[v1], 64, %[v3]\n\t"
      "v_xor_b32 %[v0], 0x60, %[v3]\n\t"
      "s_waitcnt lgkmcnt(0)\n\t"
      "global_load_dwordx2 %[av], %[v2], s[50:51]\n\t"
      "s_memrealtime s[58:59]\n\t"
      "buffer_load_dwordx4 %[v3], s[44:47], s55 offen lds\n\t"
      DMA_W("%[v4]", "s56") DMA_W("%[v1]", "s57") DMA_W("%[v0]", "s56") DMA_W("%[v3]", "s57") DMA_W("%[v4]", "s56") DMA_W("%[v1]", "s57") DMA_W("%[v0]", "s56")
      "s_branch .Lend%=\n"
      ".Latt%=:\n\t"
      "s_cmp_lt_u32 s52, 4\n\t"
      "s_cbranch_scc0 .Lcons%=\n\t"
      "s_cmp_eq_u32 s52, 1\n\t"
      "s_cbranch_scc1 .Lk%=\n\t"
      "s_cmp_eq_u32 s52, 2\n\t"
      "s_cbranch_scc1 .Lpfm%=\n\t"
      "s_lshl_b32 s55, s53, 17\n\t"
      "s_lshl_b32 s56, s54, 10\n\t"
      "s_add_u32 s55, s55, s56\n\t"
      "s_cmp_eq_u32 s52, 3\n\t"
      "s_cbranch_scc1 .Lpfs%=\n\t"
      "s_lshr_b32 s55, s53, 1\n\t"
      "s_lshl_b32 s55, s55, 17\n\t"
      "s_add_u32 s55, s55, s56\n\t"
      "s_and_b32 s56, s53, 1\n\t"
      "s_lshl_b32 s56, s56, 13\n\t"
      "s_add_u32 s55, s55, s56\n\t"
      "s_add_u32 s55, s55, 0x300000\n\t"
      "v_lshrrev_b32 %[v2], 3, %[v0]\n\t"
      "v_lshlrev_b32 %[v2], 14, %[v2]\n\t"
      "v_and_b32 %[v3], 7, %[v0]\n\t"
      "v_lshl_or_b32 %[v2], %[v3], 7, %[v2]\n\t"
      "s_branch .Lpfq%=\n"
      ".Lpfs%=:\n\t"
      "s_lshl_b32 s56, s53, 16\n\t"
      "s_add_u32 s55, s55, s56\n\t"
      "s_addk_i32 s55, 0x180\n\t"
      "v_min_u32 %[v2], 23, %[v0]\n\t"
      "v_lshlrev_b32 %[v2], 13, %[v2]\n"
      ".Lpfq%=:\n\t"
      "s_waitcnt lgkmcnt(0)\n\t"
      "buffer_load_dword %[cv], %[v2], s[44:47], s55 offen\n\t"
      "s_branch .Lk%=\n"
      ".Lpfm%=:\n\t"
      "s_mul_i32 s55, s53, 0x30000\n\t"
      "s_lshl_b32 s56, s54, 8\n\t"
      "s_add_u32 s55, s55, s56\n\t"
      "v_lshrrev_b32 %[v2], 1, %[v0]\n\t"
      "v_mul_u32_u24 %[v2], 0x1800, %[v2]\n\t"
      "v_and_b32 %[v3], 1, %[v0]\n\t"
      "v_lshl_add_u32 %[v2], %[v3], 7, %[v2]\n\t"
      "s_waitcnt lgkmcnt(0)\n\t"
      "buffer_load_dword %[cv], %[v2], s[40:43], s55 offen\n"
      ".Lk%=:\n\t"
      "s_lshl_b32 s55, s52, 15\n\t"
      "s_add_u32 m0, s55, %[lb]\n\t"
      "s_lshr_b32 s56, s53, 2\n\t"
      "s_lshl_b32 s56, s56, 3\n\t"
      "s_add_u32 s56, s56, s54\n\t"
      "s_lshl_b32 s56, s56, 4\n\t"
      "s_and_b32 s57, s53, 3\n\t"
      "s_add_u32 s56, s56, s57\n\t"
      "s_add_u32 s56, s56, 12\n\t"
      "s_lshl_b32 s56, s56, 17\n\t"
      "s_add_u32 s56, s56, s55\n\t"
      "s_mul_i32 s55, s54, 3\n\t"
      "s_lshl_b32 s57, s53, 3\n\t"
      "s_add_u32 s55, s55, s57\n\t"
      "s_and_b32 s57, s55, 1\n\t"
      "s_lshl_b32 s60, s57, 14\n\t"
      "s_add_u32 s56, s56, s60\n\t"
      "v_and_b32 %[v2], 15, %[v0]\n\t"
      "v_xor_b32 %[v2], %[v2], %[v1]\n\t"
      "v_lshlrev_b32 %[v1], 8, %[v1]\n\t"
      "v_lshl_or_b32 %[v1], %[v2], 4, %[v1]\n\t"
      "v_or_b32 %[v1], s56, %[v1]\n\t"
      "v_xor_b32 %[v2], 0x4040, %[v1]\n\t"
      "v_xor_b32 %[v3], 0x80, %[v1]\n\t"
      "v_xor_b32 %[v4], 0x40c0, %[v1]\n\t"
      "s_movk_i32 s56, 0x3c00\n\t"
      "s_add_u32 s61, s55, 1\n\t"
      "s_lshl_b32 s55, s55, 9\n\t"
      "s_lshl_b32 s61, s61, 9\n\t"
      "s_and_b32 s55, s55, s56\n\t"
      "s_and_b32 s61, s61, s56\n\t"
      "s_waitcnt lgkmcnt(0)\n\t"
      "s_memrealtime s[58:59]\n\t"
      "s_sleep " STR(K_NAP) "\n\t"
      "s_mov_b32 s57, 6\n"
      ".Lkl%=:\n\t"
      DMA_K("%[v1]", "s55") DMA_K("%[v2]", "s61") DMA_K("%[v3]", "s55") DMA_K("%[v4]", "s61")
      "s_sub_u32 s57, s57, 1\n\t"
      "s_cmp_lg_u32 s57, 0\n\t"
      "s_cbranch_scc1 .Lkl%=\n\t"
      "s_branch .Lend%=\n"
      ".Lcons%=:\n\t"
      "s_waitcnt lgkmcnt(0)\n\t"
      "s_memrealtime s[58:59]\n"
      ".Lend%=:\n\t"
      "s_waitcnt lgkmcnt(0)\n\t"
      "v_mov_b32 %[v0], s58\n\t"
      "s_nop 0"
      : [av] "=&v"(av), [cv] "=&v"(cv), [v0] "=&v"(v0_), [v1] "=&v"(v1_), [v2] "=&v"(v2_), [v3] "=&v"(v3_), [v4] "=&v"(v4_)
      : [ka] "s"(__builtin_amdgcn_kernarg_segment_ptr()), [wg] "s"(wg), [tid] "v"(tid), [lb] "s"(lbase)
      : "memory", "scc", "s36", "s37", "s38", "s39", "s40", "s41", "s42", "s43", "s44", "s45", "s46", "s47", "s48", "s49", "s50", "s51",
        "s52", "s53", "s54", "s55", "s56", "s57", "s58", "s59", "s60", "s61");
  PIN("+s"(wg));
  PIN("+v"(tid));
  const u32 t0 = __builtin_amdgcn_readfirstlane(v0_);
  const u32 x = wg & 7, r = wg >> 3;
  const u32 lane = tid & 63, w = __builtin_amdgcn_readfirstlane(tid >> 6);
  const u32 quad = lane >> 4, idx = lane & 15;
  const u32 wl = w * L_WAVE;
  const bool att = r < 16, wa = !att && r < 24;
  const u32 sl = x * 8 + (r & 7);
  const u32 bA = idx >> 2;
  const u32 kw = (w + 2 * r) & 7;
  const u32 cw = w & 3;
  const u32 cub = (((r >> 2) * 8 + x) * 8192 + 6144 + (r & 3) * 512) * 256;
  const u32 rot = 8 * r + 3 * x;
  PIN("+s"(pk), "+s"(pwm), "+s"(px), "+s"(pv), "+s"(pwo), "+s"(pw1), "+s"(pb1), "+s"(pw2), "+s"(pb2), "+s"(pout), "+s"(pws));
  const rsrc_t ws = mk_rsrc(pws, WS_BYTES);
  const u32 ep = *(const __attribute__((address_space(4))) u32*)pws;
  const u32 trq = (lane >> 2) & 3, trp = lane & 3;
  const f32x4 zero4 = {0.f, 0.f, 0.f, 0.f};

  if (att) {
    const u32 b = r >> 2, s = r & 3, g = x;
    const u32 cl = cw * 2 * L_WAVE;
    const u32 slot = WS_S4 + ((g * 4 + b) * 4 + s) * S4_SLOT;
    const u32 p2 = r & 1, j = 8 * x + (r >> 1);
    const bool ffn = x < 7;
#define HW_BAR() do { OPAQUE(); __builtin_amdgcn_s_barrier(); OPAQUE(); } while (0)
    if (w < 4) {
      const rsrc_t rk = mk_rsrc(pk, 4u * 8 * 8192 * 128 * 2), rv = mk_rsrc(pv, 4u * 8 * 8192 * 128 * 2);
      u32 o2 = rot + 24, l2 = lbase + cl + 24 * 1024;
      const u32 vk = quad * 256 + 16 * (idx ^ quad), vv0 = quad * 256 + 16 * (idx ^ (quad << 1));
#define KV_RUN(rs_, vo_, sh_, m_, cnt_) do { _Pragma("unroll 1") for (u32 i_ = 0; i_ < (cnt_); ++i_) { \
        dma<1>(rs_, (vo_) ^ ((l2 >> (sh_)) & (m_)), cub + cl + ((o2 & 1) << 14) + ((o2 & 30) << 9), l2); o2 += 1; l2 += 1024; } } while (0)
      asm volatile("s_waitcnt vmcnt(16)" ::"v"(av), "v"(cv) : "memory");
      HW_BAR();
      KV_RUN(rk, vk, 4, 192, 8);
      WAIT_VM(16); HW_BAR();
      o2 = rot; l2 = lbase + cl;
      KV_RUN(rv, vv0, 3, 128, 8);
      WAIT_VM(16); HW_BAR();
      KV_RUN(rv, vv0, 3, 128, 8);
      WAIT_VM(16); HW_BAR();
      KV_RUN(rv, vv0, 3, 128, 3);
      HW_BAR();
      KV_RUN(rv, vv0, 3, 128, 13);
      u32 rra = trp * 544 + cw * 64 + (lane >> 2) * 4, oso = slot + cw * 256 + lane * 4;
      u32 rrb = rra + 2 * L_WAVE, rrc = rra + 4 * L_WAVE, rrd = rra + 6 * L_WAVE;
      PIN("+v"(rra), "+v"(rrb), "+v"(rrc), "+v"(rrd), "+v"(oso));
      __builtin_amdgcn_s_setprio(2);
      WAIT_VM(16); HW_BAR();
      float e0, e1, e2, e3;
      u32x2 mlv;
      {
        const f32x4 u = *(LAS f32x4*)(L + L_ML + cw * 32), v = *(LAS f32x4*)(L + L_ML + cw * 32 + 16);
        const float M = vmax(vmax(u.x, u.z), vmax(v.x, v.z));
        e0 = __builtin_amdgcn_exp2f((u.x - M) * LOG2E); e1 = __builtin_amdgcn_exp2f((u.z - M) * LOG2E);
        e2 = __builtin_amdgcn_exp2f((v.x - M) * LOG2E); e3 = __builtin_amdgcn_exp2f((v.z - M) * LOG2E);
        const float Ls = (e0 * u.y + e1 * u.w) + (e2 * v.y + e3 * v.w);
        mlv = u32x2{nosent(__float_as_uint(M)), nosent(__float_as_uint(Ls))};
      }
      u32 mso = lane == 0 ? slot + 1024 + cw * 8 : WS_BYTES;
      PIN("+v"(e0), "+v"(e1), "+v"(e2), "+v"(e3), "+v"(mlv), "+v"(mso));
      WAIT_VM(8); HW_BAR();
      WAIT_VM(4); HW_BAR();
      WAIT_VM(0); HW_BAR();
      st64(ws, mso, mlv);
      HW_BAR();
      {
#define O_PAIR(a_) f32x2{*(LAS float*)(L + (a_)), *(LAS float*)(L + (a_) + 272)}
        const f32x2 v0 = O_PAIR(rra), v1 = O_PAIR(rrb), v2 = O_PAIR(rrc), v3 = O_PAIR(rrd);
        const f32x2 v = (v0 * e0 + v1 * e1) + (v2 * e2 + v3 * e3);
        __builtin_amdgcn_raw_buffer_store_b32(nosent(cvtpk(v.x, v.y)), ws, oso, 0, AUX_SC1);
      }
      SB();
      if (ffn) {
        __builtin_amdgcn_s_setprio(0);
        HW_BAR();
        const rsrc_t r1 = mk_rsrc(pw1, 512u * 1792 * 2), r2 = mk_rsrc(pw2, 1792u * 512 * 2);
        const u32 vo = (8 * (trq >> 1) + 2 * quad + (trq & 1)) * 3584 + 16 * (trp ^ ((quad & 1) << 1));
        const u32 vo2 = (8 * (lane >> 5) + ((lane >> 3) & 3)) * 1024 + 16 * ((lane & 7) ^ quad);
#pragma unroll
        for (int h = 0; h < 2; ++h) {
          const u32 f = (w + 4 * h + 2 * r) & 7;
#pragma unroll
          for (int i = 0; i < 4; ++i) dma<0>(r1, vo, (64 * f + 16 * i) * 3584 + j * 64, lbase + f * L_WAVE + 4096 + i * 1024);
        }
#pragma unroll
        for (int h = 0; h < 2; ++h) {
          const u32 f = (w + 4 * h + 4 * p2) & 7;
#pragma unroll
          for (int i = 0; i < 4; ++i) dma<0>(r2, vo2, (32 * j + 16 * (i & 1) + 4 * (i >> 1)) * 1024 + f * 128, lbase + f * L_WAVE + 8192 + i * 1024);
        }
        WAIT_VM(8);
        HW_BAR();
        WAIT_VM(0);
        HW_BAR();
        if (w == 0) { wait_heads_up(ws, x, lane, t0); HW_BAR(); }
      }
      return;
    }
    __builtin_amdgcn_s_setprio(1);
    volatile LAS u32* qflag = (volatile LAS u32*)(L + L_FLAG);
    if (w == 4) *qflag = 0;
    u32 va[8], ka_[4];
#pragma unroll
    for (int nt = 0; nt < 8; ++nt) va[nt] = cl + (4 * quad + trq) * 256 + trp * 8 + ((nt ^ (4 * (quad & 1) + trq)) << 5);
#pragma unroll
    for (int st = 0; st < 4; ++st) ka_[st] = cl + idx * 256 + 16 * ((4 * st + quad) ^ idx);
    u32 qa = L_Q + (idx >> 2) * 256 + quad * 16, mlw = L_ML + (lane >> 2) * 32 + cw * 8, rda = cl + lane * 4;
    u32 qo = WS_S2 + (b * 8 + g) * 2048 + lane * 16, fo = WS_FLAG + lane * 8;
    PIN("+v"(va[0]), "+v"(va[1]), "+v"(va[2]), "+v"(va[3]), "+v"(va[4]), "+v"(va[5]), "+v"(va[6]), "+v"(va[7]));
    PIN("+v"(ka_[0]), "+v"(ka_[1]), "+v"(ka_[2]), "+v"(ka_[3]), "+v"(qo), "+v"(fo), "+v"(qa), "+v"(mlw), "+v"(rda));
    const u32 bb = (lane >> 3) & 1, bq = 2 * p2 + bb, ch = lane & 7;
    const u32 kk = 128 * cw + 8 * ch + 2 * quad;
    u32 so3 = WS_S3 + (kk >> 5) * 256 + bq * 64 + (kk & 31) * 2, xo = (bq * 512 + kk) * 2, b1o = ((ffn ? 32 * j : 0) + 8 * quad + ch) * 2;
    PIN("+v"(so3), "+v"(xo), "+v"(b1o));
    TAKE_PROLOGUE(0);
    u32x4 kf[8][4];
    u32x4 eq0 = {0u, 0u, 0u, 0u}, eq1 = eq0, fq0 = eq0, fq1 = eq0; u32x2 ef = {0u, 0u}, ff = ef;
#define Q_ASK(a_, b_, f_) \
    asm volatile("buffer_load_dwordx2 %2, %4, %5, 0 offen sc1\n\tbuffer_load_dwordx4 %0, %3, %5, 0 offen sc1\n\t" \
                 "buffer_load_dwordx4 %1, %3, %5, 0 offen offset:1024 sc1" \
                 : "+v"(a_), "+v"(b_), "+v"(f_) : "v"(qo), "v"(fo), "s"(ws) : "memory")
#define Q_TAKE(N, a_, b_, f_) \
    asm volatile("s_waitcnt vmcnt(" #N ")\n\tv_mov_b32 %0, %5\n\tv_mov_b32 %1, %6\n\tv_mov_b32 %2, %7\n\tv_mov_b32 %3, %8\n\t" \
                 "v_xor_b32 %4, %9, %15\n\tv_bitop3_b32 %4, %10, %15, %4 bitop3:0xbe\n\tv_bitop3_b32 %4, %11, %15, %4 bitop3:0xbe\n\t" \
                 "v_bitop3_b32 %4, %12, %15, %4 bitop3:0xbe\n\tv_bitop3_b32 %4, %13, %15, %4 bitop3:0xbe\n\tv_bitop3_b32 %4, %14, %15, %4 bitop3:0xeb" \
                 : "=&v"(q0.x), "=&v"(q0.z), "=&v"(q1.x), "=&v"(q1.z), "=&v"(bad0) \
                 : "v"(a_.x), "v"(a_.z), "v"(b_.x), "v"(b_.z), "v"(a_.y), "v"(a_.w), "v"(b_.y), "v"(b_.w), "v"(f_.x), "v"(f_.y), "v"(tag) : "memory")
#pragma unroll
    for (int u = 0; u < 4; ++u) {
      if (u == 3 && w == 4) {
        wait_until(t0, T_QPOLL);
        Q_ASK(eq0, eq1, ef);
      }
      BARRIER();
      if (u == 3 && w == 4) Q_ASK(fq0, fq1, ff);
#pragma unroll
      for (int t = 2 * u; t < 2 * u + 2; ++t) {
#pragma unroll
        for (int st = 0; st < 4; ++st) kf[t][st] = *(LAS u32x4*)(L + ka_[st] + t * 4096);
      }
#pragma unroll
      for (int t = 2 * u; t < 2 * u + 2; ++t) PIN("+v"(kf[t][0]), "+v"(kf[t][1]), "+v"(kf[t][2]), "+v"(kf[t][3]));
    }
    BARRIER();
    if (w == 4) {
      u32x4 q0, q1;
      u32 bad0;
      Q_TAKE(3, eq0, eq1, ef);
      if (!none(bad0 != 0)) Q_TAKE(0, fq0, fq1, ff);
      if (!none(bad0 != 0)) {
        POLL_BEGIN
          const u32x2 f = ld64(ws, fo);
          q0 = ld128(ws, qo); q1 = ld128(ws, qo + 1024);
          const u32 bad = ((q0.y ^ tag) | (q0.w ^ tag)) | ((q1.y ^ tag) | (q1.w ^ tag)) | ((f.x ^ tag) | (f.y ^ ~tag));
        POLL_END(bad != 0, (void)0)
      }
      *(LAS u32x2*)(L + L_Q + lane * 8) = u32x2{q0.x, q0.z};
      *(LAS u32x2*)(L + L_Q + 512 + lane * 8) = u32x2{q1.x, q1.z};
      OPAQUE();
      *qflag = 1;
    } else {
      while (__builtin_amdgcn_readfirstlane(*qflag) == 0) __builtin_amdgcn_s_sleep(1);
    }
    OPAQUE();
    float sc[8];
    {
      u32x4 qf[4];
#pragma unroll
      for (int st = 0; st < 4; ++st) qf[st] = *(LAS u32x4*)(L + qa + st * 64);
      SB();
#pragma unroll
      for (int hf = 0; hf < 2; ++hf) {
        f32x4 sacc[4];
#pragma unroll
        for (int st = 0; st < 4; ++st)
#pragma unroll
          for (int t = 0; t < 4; ++t) sacc[t] = mfma(kf[4 * hf + t][st], qf[st], st ? sacc[t] : zero4);
#pragma unroll
        for (int e = 0; e < 4; ++e) {
          const float lo = (idx & 1) ? sacc[1][e] : sacc[0][e], hi = (idx & 1) ? sacc[3][e] : sacc[2][e];
          sc[4 * hf + e] = (idx & 2) ? hi : lo;
        }
      }
    }
#pragma unroll
    for (int e = 0; e < 8; e += 2) {
      constexpr float SCALE = 0.08838834764831845f;
      const u32 a = cvtpk(sc[e], sc[e + 1]), a2 = cvtpk(bflo(a) * SCALE, bfhi(a) * SCALE);
      sc[e] = bflo(a2); sc[e + 1] = bfhi(a2);
    }
    float mw = vmax(vmax(vmax(sc[0], sc[1]), vmax(sc[2], sc[3])), vmax(vmax(sc[4], sc[5]), vmax(sc[6], sc[7])));
    mw = vmax(mw, dppf<XOR1>(mw)); mw = vmax(mw, dppf<XOR2>(mw)); mw = mx16(mw); mw = mx32(mw);
    u32x4 pa[4];
    {
      float p[8];
#pragma unroll
      for (int e = 0; e < 8; ++e) p[e] = __builtin_amdgcn_exp2f((sc[e] - mw) * LOG2E);
      float lw = ((p[0] + p[1]) + (p[2] + p[3])) + ((p[4] + p[5]) + (p[6] + p[7]));
      lw += dppf<XOR1>(lw); lw += dppf<XOR2>(lw); lw = rs16(lw, lw); lw = rs32(lw, lw);
      if ((lane & 0x33) == 0) *(LAS f32x2*)(L + mlw) = f32x2{mw, lw};
#pragma unroll
      for (int hf = 0; hf < 2; ++hf) {
        const u32 pk0 = cvtpk(p[4 * hf], p[4 * hf + 1]), pk1 = cvtpk(p[4 * hf + 2], p[4 * hf + 3]);
        pa[2 * hf] = u32x4{dppu<QP(0, 0, 0, 0)>(pk0), dppu<QP(0, 0, 0, 0)>(pk1), dppu<QP(1, 1, 1, 1)>(pk0), dppu<QP(1, 1, 1, 1)>(pk1)};
        pa[2 * hf + 1] = u32x4{dppu<QP(2, 2, 2, 2)>(pk0), dppu<QP(2, 2, 2, 2)>(pk1), dppu<QP(3, 3, 3, 3)>(pk0), dppu<QP(3, 3, 3, 3)>(pk1)};
      }
    }
    PIN("+v"(pa[0]), "+v"(pa[1]), "+v"(pa[2]), "+v"(pa[3]));
    BARRIER();
    f32x4 oacc[8];
    u32 es[9];
    {
      u32x4 vf[8], vg[8];
#pragma unroll
      for (int nt = 0; nt < 8; ++nt) vf[nt] = tr_frag(L, va[nt], va[nt] + 4096);
#pragma unroll
      for (int nt = 0; nt < 8; ++nt) vg[nt] = tr_frag(L, va[nt] + 8192, va[nt] + 12288);
      SB();
#pragma unroll
      for (int nt = 0; nt < 8; ++nt) oacc[nt] = mfma(pa[0], vf[nt], zero4);
#pragma unroll
      for (int nt = 0; nt < 8; ++nt) oacc[nt] = mfma(pa[1], vg[nt], oacc[nt]);
      SB();
      asm volatile("buffer_load_dword %0, %9, %12, 0 offen sc1\n\tbuffer_load_dword %1, %9, %12, 0 offen offset:512 sc1\n\t"
                   "buffer_load_dword %2, %9, %12, %13 offen sc1\n\tbuffer_load_dword %3, %9, %12, %13 offen offset:512 sc1\n\t"
                   "buffer_load_dword %4, %9, %12, %14 offen sc1\n\tbuffer_load_dword %5, %9, %12, %14 offen offset:512 sc1\n\t"
                   "global_load_dword %6, %10, %15\n\tglobal_load_dword %7, %10, %15 offset:128\n\tglobal_load_ushort %8, %11, %16"
                   : "=&v"(es[0]), "=&v"(es[1]), "=&v"(es[2]), "=&v"(es[3]), "=&v"(es[4]), "=&v"(es[5]), "=&v"(es[6]), "=&v"(es[7]), "=&v"(es[8])
                   : "v"(so3), "v"(xo), "v"(b1o), "s"(ws), "s"(4096u), "s"(8192u), "s"(px), "s"(pb1) : "memory");
      SB();
      HW_BAR();
#pragma unroll
      for (int nt = 0; nt < 8; ++nt) vf[nt] = tr_frag(L, va[nt] + 16384, va[nt] + 20480);
      SB();
#pragma unroll
      for (int nt = 0; nt < 8; ++nt) oacc[nt] = mfma(pa[2], vf[nt], oacc[nt]);
      SB();
      asm volatile(
          "s_barrier\n\t"
          "ds_read_b64_tr_b16 v[128:129], %[a0] offset:24576\n\t"
          "ds_read_b64_tr_b16 v[132:133], %[a1] offset:24576\n\t"
          "ds_read_b64_tr_b16 v[136:137], %[a2] offset:24576\n\t"
          "ds_read_b64_tr_b16 v[140:141], %[a3] offset:24576\n\t"
          "ds_read_b64_tr_b16 v[144:145], %[a4] offset:24576\n\t"
          "ds_read_b64_tr_b16 v[148:149], %[a5] offset:24576\n\t"
          "ds_read_b64_tr_b16 v[152:153], %[a6] offset:24576\n\t"
          "ds_read_b64_tr_b16 v[156:157], %[a7] offset:24576\n\t"
          "s_barrier\n\t"
          "ds_read_b64_tr_b16 v[130:131], %[a0] offset:28672\n\t"
          "ds_read_b64_tr_b16 v[134:135], %[a1] offset:28672\n\t"
          "ds_read_b64_tr_b16 v[138:139], %[a2] offset:28672\n\t"
          "ds_read_b64_tr_b16 v[142:143], %[a3] offset:28672\n\t"
          "ds_read_b64_tr_b16 v[146:147], %[a4] offset:28672\n\t"
          "ds_read_b64_tr_b16 v[150:151], %[a5] offset:28672\n\t"
          "ds_read_b64_tr_b16 v[154:155], %[a6] offset:28672\n\t"
          "ds_read_b64_tr_b16 v[158:159], %[a7] offset:28672\n\t"
          "s_waitcnt lgkmcnt(7)\n\t" MFMA "v[128:131], %[pa], v[128:131], %[c0]\n\t"
          "s_waitcnt lgkmcnt(6)\n\t" MFMA "v[132:135], %[pa], v[132:135], %[c1]\n\t"
          "s_waitcnt lgkmcnt(5)\n\t" MFMA "v[136:139], %[pa], v[136:139], %[c2]\n\t"
          "s_waitcnt lgkmcnt(4)\n\t" MFMA "v[140:143], %[pa], v[140:143], %[c3]\n\t"
          "s_waitcnt lgkmcnt(3)\n\t" MFMA "v[144:147], %[pa], v[144:147], %[c4]\n\t"
          "s_waitcnt lgkmcnt(2)\n\t" MFMA "v[148:151], %[pa], v[148:151], %[c5]\n\t"
          "s_waitcnt lgkmcnt(1)\n\t" MFMA "v[152:155], %[pa], v[152:155], %[c6]\n\t"
          "s_waitcnt lgkmcnt(0)\n\t" MFMA "v[156:159], %[pa], v[156:159], %[c7]\n\t"
          "ds_write2_b32 %[w0], v128, v132 offset1:68\n\t"
          "ds_write2_b32 %[w0], v136, v140 offset0:136 offset1:204\n\t"
          "s_nop 1\n\t"
          "ds_write2_b32 %[w1], v144, v148 offset1:68\n\t"
          "s_nop 2\n\t"
          "ds_write2_b32 %[w1], v152, v156 offset0:136 offset1:204\n\t"
          "s_waitcnt lgkmcnt(0)\n\t"
          "s_barrier"
          :
          : [a0] "v"(lbase + va[0]), [a1] "v"(lbase + va[1]), [a2] "v"(lbase + va[2]), [a3] "v"(lbase + va[3]), [a4] "v"(lbase + va[4]),
            [a5] "v"(lbase + va[5]), [a6] "v"(lbase + va[6]), [a7] "v"(lbase + va[7]),
            [pa] "v"(pa[3]), [c0] "v"(oacc[0]), [c1] "v"(oacc[1]), [c2] "v"(oacc[2]), [c3] "v"(oacc[3]), [c4] "v"(oacc[4]), [c5] "v"(oacc[5]),
            [c6] "v"(oacc[6]), [c7] "v"(oacc[7]), [w0] "v"(lbase + rda), [w1] "v"(lbase + rda + 1088)
          : "memory", "v128", "v129", "v130", "v131", "v132", "v133", "v134", "v135", "v136", "v137", "v138", "v139", "v140", "v141", "v142", "v143",
            "v144", "v145", "v146", "v147", "v148", "v149", "v150", "v151", "v152", "v153", "v154", "v155", "v156", "v157", "v158", "v159");
    }
    u32 gav[2], sbv[2], cbv[2], xv0, xv1, b1v;
    asm volatile("s_waitcnt vmcnt(0)\n\tv_mov_b32 %0, %9\n\tv_mov_b32 %1, %10\n\tv_mov_b32 %2, %11\n\tv_mov_b32 %3, %12\n\tv_mov_b32 %4, %13\n\t"
                 "v_mov_b32 %5, %14\n\tv_mov_b32 %6, %15\n\tv_mov_b32 %7, %16\n\tv_mov_b32 %8, %17"
                 : "=&v"(gav[0]), "=&v"(gav[1]), "=&v"(sbv[0]), "=&v"(sbv[1]), "=&v"(cbv[0]), "=&v"(cbv[1]), "=&v"(xv0), "=&v"(xv1), "=&v"(b1v)
                 : "v"(es[0]), "v"(es[1]), "v"(es[2]), "v"(es[3]), "v"(es[4]), "v"(es[5]), "v"(es[6]), "v"(es[7]), "v"(es[8]),
                   "v"(fq0), "v"(fq1), "v"(ff) : "memory");
    if (ffn) {
      HW_BAR();
#define S3_BAD (umax(umax(umax(gav[0], sbv[0]), cbv[0]), umax(umax(gav[1], sbv[1]), cbv[1])) == SENT)
      if (!none(S3_BAD)) {
        POLL_BEGIN
#pragma unroll
          for (int h = 0; h < 2; ++h) { gav[h] = ld32(ws, so3 + h * 512); sbv[h] = ld32(ws, so3 + 4096 + h * 512); cbv[h] = ld32(ws, so3 + 8192 + h * 512); }
        POLL_END(S3_BAD, __builtin_amdgcn_s_sleep(2))
      }
      u32 rw = L_RED + (2 * cw + (quad & 1)) * 256 + (quad >> 1) * 128 + idx * 4, rr = L_RED + bb * 128 + (8 * quad + ch) * 4;
      const u32 c1v0 = cvtpk(1.f + bflo(cbv[0]), 1.f + bfhi(cbv[0])), c1v1 = cvtpk(1.f + bflo(cbv[1]), 1.f + bfhi(cbv[1]));
      f32x2 xa = unpk(xv0), xb = unpk(xv1), ga = unpk(gav[0]), gb_ = unpk(gav[1]);
      f32x2 xca = xa * unpk(c1v0), xcb = xb * unpk(c1v1), gca = ga * unpk(c1v0), gcb = gb_ * unpk(c1v1);
      u32x4 cA = quad_frag(c1v0), cB = quad_frag(c1v1), sA = quad_frag(sbv[0]), sB = quad_frag(sbv[1]);
      float b1f = bflo(b1v);
      u32x4 sel = pair_selector(idx);
      u32 so5 = WS_S5 + (x & 3) * 32768 + (cw * 16 + quad) * 512 + p2 * 256 + idx * 16;
      u32 stw = lbase + (ch ? L_RED + 6144 + (tid & 255) * 4 : L_ST + bb * 64 + (cw * 2 + (quad & 1)) * 8 + (lane >> 5) * 4), str = lbase + L_ST + bb * 64;
      u32 yw = lbase + L_RED + ((quad >> 1) * 32 + idx) * 32 + (2 * cw + (quad & 1)) * 4, yr = lbase + L_RED + (bb * 32 + 8 * quad + ch) * 32;
      u32 so6 = (quad & 1) ? WS_BYTES : WS_S6 + (((2 * p2 + (quad >> 1)) * 16 + 4 * cw + (idx >> 2)) * 56 + j) * 128 + trp * 16;
      u32 t1a[2], t2a[4];
#pragma unroll
      for (int nt = 0; nt < 2; ++nt) t1a[nt] = cl + 4096 + (4 * quad + trq) * 64 + ((nt ^ (quad & 1)) << 5) + trp * 8;
#pragma unroll
      for (int nt = 0; nt < 4; ++nt)
        t2a[nt] = cl + 8192 + (trp >> 1) * (L_WAVE + 8) + (4 * quad + trq) * 128 + (((4 * (trp & 1) + nt) ^ (2 * (quad & 1) + (trq >> 1))) << 4);
      PIN("+v"(xa), "+v"(xb), "+v"(ga), "+v"(gb_), "+v"(xca), "+v"(xcb), "+v"(gca), "+v"(gcb), "+v"(sel), "+v"(b1f));
      PIN("+v"(cA), "+v"(cB), "+v"(sA), "+v"(sB), "+v"(t1a[0]), "+v"(t1a[1]), "+v"(t2a[0]), "+v"(t2a[1]), "+v"(t2a[2]), "+v"(t2a[3]));
      PIN("+v"(so5), "+v"(stw), "+v"(str), "+v"(rw), "+v"(rr), "+v"(so6), "+v"(yw), "+v"(yr));
      __builtin_amdgcn_s_barrier();
      OPAQUE();
      u32x4 f1[2][2][2], f2[2][4];
#pragma unroll
      for (int h = 0; h < 2; ++h)
#pragma unroll
        for (int nt = 0; nt < 2; ++nt)
#pragma unroll
          for (int t = 0; t < 2; ++t) f1[h][t][nt] = tr_frag(L, t1a[nt] + h * L_WAVE + t * 2048, t1a[nt] + h * L_WAVE + t * 2048 + 1024);
      SB();
#define FFN_W1(hA_, hB_, dst) do { \
        f32x4 d00 = mfma(hA_, f1[0][0][0], zero4), d10 = mfma(hA_, f1[0][1][0], zero4), d01 = mfma(hA_, f1[0][0][1], zero4), d11 = mfma(hA_, f1[0][1][1], zero4); \
        d00 = mfma(hB_, f1[1][0][0], d00); d10 = mfma(hB_, f1[1][1][0], d10); d01 = mfma(hB_, f1[1][0][1], d01); d11 = mfma(hB_, f1[1][1][1], d11); \
        *(LAS float*)(L + rw + (dst)) = (quad & 1) ? d10.x : d00.x; \
        *(LAS float*)(L + rw + (dst) + 64) = (quad & 1) ? d11.x : d01.x; } while (0)
#define FFN_SUM8(src) (((*(LAS float*)(L + rr + (src)) + *(LAS float*)(L + rr + (src) + 256)) + (*(LAS float*)(L + rr + (src) + 512) + *(LAS float*)(L + rr + (src) + 768))) + \
                       ((*(LAS float*)(L + rr + (src) + 1024) + *(LAS float*)(L + rr + (src) + 1280)) + (*(LAS float*)(L + rr + (src) + 1536) + *(LAS float*)(L + rr + (src) + 1792))))
      FFN_W1(cA, cB, 2048);
      FFN_W1(sA, sB, 4096);
      BARRIER();
#pragma unroll
      for (int h = 0; h < 2; ++h)
#pragma unroll
        for (int nt = 0; nt < 4; ++nt) {
          const u32 a = t2a[2 * h + (nt >> 1)] ^ ((nt & 1) * 8);
          f2[h][nt] = tr_frag(L, a, a + 2048);
        }
      float uu = FFN_SUM8(2048), vv = FFN_SUM8(4096) + b1f;
      PIN("+v"(f2[0][0]), "+v"(f2[0][1]), "+v"(f2[0][2]), "+v"(f2[0][3]), "+v"(f2[1][0]), "+v"(f2[1][1]), "+v"(f2[1][2]), "+v"(f2[1][3]));
      f32x2 inv2 = {1.f / 512.f, 1.f / 512.f}, eps2 = {0.f, 1e-5f};
      PIN("+v"(uu), "+v"(vv), "+v"(inv2), "+v"(eps2));
      HW_BAR();
      {
        u32 spin = SPIN_CAP;
        asm volatile(
            ".Lask%=:\n\t"
            "buffer_load_dwordx4 v[128:131], %[so5], %[ws], 0 offen sc1\n\t"
            "buffer_load_dwordx4 v[132:135], %[so5], %[ws], 0 offen offset:2048 sc1\n\t"
            "buffer_load_dwordx4 v[136:139], %[so5], %[ws], %[c4k] offen sc1\n\t"
            "buffer_load_dwordx4 v[140:143], %[so5], %[ws], %[c4k] offen offset:2048 sc1\n\t"
            "s_waitcnt vmcnt(3)\n\t"
            MFMA "v[144:147], %[sel], v[128:131], 0\n\t"
            "v_max3_u32 v152, v128, v129, v130\n\t"
            "s_waitcnt vmcnt(2)\n\t"
            MFMA "v[144:147], %[sel], v[132:135], v[144:147]\n\t"
            "v_max3_u32 v152, v131, v132, v152\n\t"
            "v_max3_u32 v153, v133, v134, v135\n\t"
            "s_waitcnt vmcnt(1)\n\t"
            MFMA "v[148:151], %[sel], v[136:139], 0\n\t"
            "v_max3_u32 v152, v136, v137, v152\n\t"
            "v_max3_u32 v153, v138, v139, v153\n\t"
            "s_waitcnt vmcnt(0)\n\t"
            MFMA "v[148:151], %[sel], v[140:143], v[148:151]\n\t"
            "v_max3_u32 v152, v140, v141, v152\n\t"
            "v_pk_fma_f32 v[154:155], %[gca], v[144:145], %[xca]\n\t"
            "v_max3_u32 v153, v142, v143, v153\n\t"
            "v_cvt_pk_bf16_f32 v156, v154, v155\n\t"
            "v_max_u32 v152, v152, v153\n\t"
            "v_cmp_eq_u32 vcc, -1, v152\n\t"
            DPPQ("v160", "v156", "0") DPPQ("v161", "v156", "1") DPPQ("v162", "v156", "2") DPPQ("v163", "v156", "3")
            "v_pk_fma_f32 v[158:159], %[gcb], v[148:149], %[xcb]\n\t"
            "s_cbranch_vccz .Lgo%=\n\t"
            "s_sub_u32 %[spin], %[spin], 1\n\t"
            "s_cbranch_scc0 .Lask%=\n"
            ".Lgo%=:\n\t"
            MFMA "v[168:171], v[160:163], %[f000], 0\n\t"
            "v_cvt_pk_bf16_f32 v157, v158, v159\n\t"
            MFMA "v[172:175], v[160:163], %[f010], 0\n\t"
            "v_pk_fma_f32 v[154:155], %[ga], v[144:145], %[xa]\n\t"
            DPPQ("v164", "v157", "0")
            MFMA "v[176:179], v[160:163], %[f001], 0\n\t"
            DPPQ("v165", "v157", "1") DPPQ("v166", "v157", "2")
            MFMA "v[180:183], v[160:163], %[f011], 0\n\t"
            DPPQ("v167", "v157", "3")
            "v_pk_fma_f32 v[158:159], %[gb], v[148:149], %[xb]\n\t"
            "v_pk_mul_f32 v[152:153], v[154:155], v[154:155]\n\t"
            MFMA "v[168:171], v[164:167], %[f100], v[168:171]\n\t"
            "v_pk_add_f32 v[154:155], v[154:155], v[158:159]\n\t"
            "v_pk_fma_f32 v[152:153], v[158:159], v[158:159], v[152:153]\n\t"
            MFMA "v[172:175], v[164:167], %[f110], v[172:175]\n\t"
            "v_add_f32 v154, v154, v155\n\t"
            "v_add_f32 v152, v152, v153\n\t"
            MFMA "v[176:179], v[164:167], %[f101], v[176:179]\n\t"
            "s_nop 0\n\t"
            "v_permlane32_swap_b32 v154, v152\n\t"
            MFMA "v[180:183], v[164:167], %[f111], v[180:183]\n\t"
            "v_add_f32 v154, v154, v152\n\t"
            "s_nop 0\n\t"
            "v_cndmask_b32 v156, v168, v172, %[odd]\n\t"
            "v_add_f32_dpp v154, v154, v154 quad_perm:[1,0,3,2] row_mask:0xf bank_mask:0xf\n\t"
            "ds_write_b32 %[yw], v156\n\t"
            "s_nop 0\n\t"
            "v_add_f32_dpp v154, v154, v154 quad_perm:[2,3,0,1] row_mask:0xf bank_mask:0xf\n\t"
            "s_nop 0\n\t"
            "v_cndmask_b32 v157, v176, v180, %[odd]\n\t"
            "v_add_f32_dpp v154, v154, v154 row_half_mirror row_mask:0xf bank_mask:0xf\n\t"
            "ds_write_b32 %[yw], v157 offset:512\n\t"
            "ds_write_b32 %[stw], v154\n\t"
            "s_waitcnt lgkmcnt(0)\n\t"
            "s_barrier"
            : [spin] "+s"(spin)
            : [so5] "v"(so5), [ws] "s"(ws), [c4k] "s"(4096u), [sel] "v"(sel), [gca] "v"(gca), [xca] "v"(xca), [gcb] "v"(gcb), [xcb] "v"(xcb),
              [ga] "v"(ga), [xa] "v"(xa), [gb] "v"(gb_), [xb] "v"(xb), [f000] "v"(f1[0][0][0]), [f010] "v"(f1[0][1][0]), [f001] "v"(f1[0][0][1]),
              [f011] "v"(f1[0][1][1]), [f100] "v"(f1[1][0][0]), [f110] "v"(f1[1][1][0]), [f101] "v"(f1[1][0][1]), [f111] "v"(f1[1][1][1]),
              [odd] "s"(0xFFFF0000FFFF0000ull), [yw] "v"(yw), [stw] "v"(stw)
            : "memory", "vcc", "scc", TMP_CLOBBERS);
        asm volatile(
            "ds_read_b128 v[128:131], %[str]\n\t"
            "ds_read_b128 v[132:135], %[str] offset:16\n\t"
            "ds_read_b128 v[136:139], %[str] offset:32\n\t"
            "ds_read_b128 v[140:143], %[str] offset:48\n\t"
            "ds_read_b128 v[144:147], %[yr]\n\t"
            "ds_read_b128 v[148:151], %[yr] offset:16\n\t"
            "s_waitcnt lgkmcnt(4)\n\t"
            "v_pk_add_f32 v[128:129], v[128:129], v[130:131]\n\t"
            "v_pk_add_f32 v[132:133], v[132:133], v[134:135]\n\t"
            "s_waitcnt lgkmcnt(2)\n\t"
            "v_pk_add_f32 v[136:137], v[136:137], v[138:139]\n\t"
            "v_pk_add_f32 v[140:141], v[140:141], v[142:143]\n\t"
            "v_pk_add_f32 v[128:129], v[128:129], v[132:133]\n\t"
            "s_waitcnt lgkmcnt(0)\n\t"
            "v_pk_add_f32 v[136:137], v[136:137], v[140:141]\n\t"
            "v_pk_add_f32 v[144:145], v[144:145], v[148:149]\n\t"
            "v_pk_add_f32 v[128:129], v[128:129], v[136:137]\n\t"
            "v_pk_add_f32 v[146:147], v[146:147], v[150:151]\n\t"
            "v_pk_fma_f32 v[128:129], v[128:129], %[inv], %[eps2]\n\t"
            "v_pk_add_f32 v[144:145], v[144:145], v[146:147]\n\t"
            "v_fma_f32 v129, -v128, v128, v129\n\t"
            "v_add_f32 v144, v144, v145\n\t"
            "v_max_f32 v129, 0x3727c5ac, v129\n\t"
            "v_rsq_f32 v129, v129\n\t"
            "v_fma_f32 v144, -v128, %[uu], v144\n\t"
            "v_fma_f32 v144, v144, v129, %[vv]\n\t"
            "v_max_f32 v144, 0, v144\n\t"
            "v_mul_f32 v144, v144, v144\n\t"
            "s_nop 1\n\t"
            "v_mov_b32_dpp v145, v144 quad_perm:[1,0,3,2] row_mask:0xf bank_mask:0xf\n\t"
            "v_cvt_pk_bf16_f32 v132, v144, v145\n\t"
            "s_nop 1\n\t"
            "v_mov_b32_dpp v133, v132 row_shl:2 row_mask:0xf bank_mask:0xf bound_ctrl:1\n\t"
            "v_mov_b32_dpp v134, v132 row_shl:4 row_mask:0xf bank_mask:0xf bound_ctrl:1\n\t"
            "v_mov_b32_dpp v135, v132 row_shl:6 row_mask:0xf bank_mask:0xf bound_ctrl:1\n\t"
            "s_nop 1\n\t"
            MFMA "v[152:155], v[132:135], %[g0], 0\n\t"
            MFMA "v[156:159], v[132:135], %[g1], 0\n\t"
            MFMA "v[160:163], v[132:135], %[g2], 0\n\t"
            MFMA "v[164:167], v[132:135], %[g3], 0\n\t"
            MFMA "v[168:171], v[132:135], %[g4], 0\n\t"
            MFMA "v[172:175], v[132:135], %[g5], 0\n\t"
            MFMA "v[176:179], v[132:135], %[g6], 0\n\t"
            MFMA "v[180:183], v[132:135], %[g7], 0\n\t"
            "s_nop 1\n\t"
            "v_cvt_pk_bf16_f32 v136, v152, v156\n\t"
            "v_min_u32 v136, -2, v136\n\t"
            "v_cvt_pk_bf16_f32 v137, v160, v164\n\t"
            "v_min_u32 v137, -2, v137\n\t"
            "v_cvt_pk_bf16_f32 v138, v168, v172\n\t"
            "v_min_u32 v138, -2, v138\n\t"
            "v_cvt_pk_bf16_f32 v139, v176, v180\n\t"
            "v_min_u32 v139, -2, v139\n\t"
            "buffer_store_dwordx4 v[136:139], %[so6], %[ws], 0 offen sc1\n\t"
            "s_nop 1"
            :
            : [str] "v"(str), [yr] "v"(yr), [inv] "v"(inv2), [eps2] "v"(eps2), [uu] "v"(uu), [vv] "v"(vv), [g0] "v"(f2[0][0]), [g1] "v"(f2[0][1]),
              [g2] "v"(f2[0][2]), [g3] "v"(f2[0][3]), [g4] "v"(f2[1][0]), [g5] "v"(f2[1][1]), [g6] "v"(f2[1][2]), [g7] "v"(f2[1][3]),
              [so6] "v"(so6), [ws] "s"(ws)
            : "memory", TMP_CLOBBERS);
      }
    }
    return;
  }

  if (wa) {
    const u32 ra = r - 16;
    const u32 mi = ((ra >> 2) * 16 + x * 2 + (ra & 1)) * 2 + ((ra >> 1) & 1);
    const u32 rid = ra * 8 + x;
    const rsrc_t rm = mk_rsrc(pwm, 512u * 3072 * 2);
    TAKE_PROLOGUE(2);
    u32x4 sa;
    {
      float f[4] = {bflo(av0), bfhi(av0), bflo(av1), bfhi(av1)};
#pragma unroll
      for (int e = 0; e < 4; ++e) f[e] = f[e] * __builtin_amdgcn_rcpf(1.f + __builtin_amdgcn_exp2f(-f[e] * LOG2E));
      sa = pair_frag(cvtpk(f[0], f[1]), cvtpk(f[2], f[3]));
    }
    u32 ta = wl + (4 * quad + trq) * 32 + trp * 8, rw = L_RED + kw * 256 + lane * 4, rr = L_RED + quad * 512 + idx * 16;
    u32 so1 = WS_S1 + sl * 256 + lane * 16;
    PIN("+v"(sa), "+v"(ta), "+v"(rw), "+v"(rr), "+v"(so1), "+v"(tag));
    WAIT_VM(0);
    {
      const u32x4 w0 = tr_frag(L, ta, ta + 512), w1 = tr_frag(L, ta + 1024, ta + 1536);
      SB();
      const f32x4 d0 = mfma(sa, w0, zero4), d1 = mfma(sa, w1, zero4);
      *(LAS float*)(L + rw) = d0.x + d1.z;
    }
    BARRIER();
    if (w == 0) {
      const f32x4 v = *(LAS f32x4*)(L + rr) + *(LAS f32x4*)(L + rr + 256);
      float e0 = rs16(v.x, v.x), e1 = rs16(v.y, v.y), e2 = rs16(v.z, v.z), e3 = rs16(v.w, v.w);
      e0 = rs32(e0, e0); e1 = rs32(e1, e1); e2 = rs32(e2, e2); e3 = rs32(e3, e3);
      if (lane < 16) st128(ws, so1, u32x4{cvtpk(e0, e1), tag, cvtpk(e2, e3), tag});
    }
    SB();
    __builtin_amdgcn_s_barrier();
    {
      const u32x4 sv = {SENT, SENT, SENT, SENT};
      st128(ws, WS_S3 + rid * CLR + tid * 16, sv);
      if (tid * 16 < CLR - 8192) st128(ws, WS_S3 + rid * CLR + 8192 + tid * 16, sv);
      WAIT_VM(0);
      __builtin_amdgcn_s_barrier();
      if (tid == 0) st64(ws, WS_FLAG + rid * 8, u32x2{tag, ~tag});
    }
    {
      const u32 vo = (lane >> 2) * 6144 + 16 * (trp ^ ((quad & 1) << 1));
      const u32 kd = (w + 2 * (r & 2)) & 7;
#pragma unroll
      for (int i = 0; i < 4; ++i) dma<0>(rm, vo, (64 * kd + 16 * i) * 6144 + 2048 + mi * 64, lbase + ((kd - 2 * r) & 7) * L_WAVE + 4096 + i * 1024);
    }
    WAIT_VM(0);
    __builtin_amdgcn_s_barrier();
    OPAQUE();
#pragma unroll
    for (int nt = 0; nt < 2; ++nt) {
      const u32 a = wl + 4096 + (4 * quad + trq) * 64 + ((nt ^ (quad & 1)) << 5) + trp * 8;
      const f32x4 d0 = mfma(sa, tr_frag(L, a, a + 1024), zero4), d1 = mfma(sa, tr_frag(L, a + 2048, a + 3072), zero4);
      *(LAS float*)(L + L_RED + 2048 + kw * 512 + quad * 128 + nt * 64 + idx * 4) = d0.x + d1.z;
    }
    if (w == 0) wait_flags(ws, lane, tag);
    BARRIER();
    if (w != 0) {
      if (w < 5) {
        const u32 pu = ra * 4 + w - 1, po = ((pu >> 4) * 128 + (pu & 15) + 16 * (lane >> 3)) * 1024 + (lane & 7) * 128;
        u32 d0, d1;
        wait_until(t0, T_WO);
        asm volatile("global_load_dword %0, %1, %2\n\ts_waitcnt vmcnt(0)" : "=&v"(d0) : "v"((((x + 1) & 7) * 512 + 256) * 1024 + po), "s"(pwo) : "memory");
        if (x < 7)
          asm volatile("global_load_dword %0, %2, %3\n\tglobal_load_dword %1, %4, %5\n\ts_waitcnt vmcnt(0)" : "=&v"(d0), "=&v"(d1)
                       : "v"((pu * 16 + (lane >> 2)) * 3584 + x * 512 + (lane & 3) * 128), "s"(pw1), "v"(x * 256 * 1024 + po), "s"(pw2) : "memory");
      }
      return;
    }
    {
      f32x4 v = zero4;
#pragma unroll
      for (int i = 0; i < 8; ++i) v += *(LAS f32x4*)(L + L_RED + 2048 + i * 512 + (lane & 31) * 16);
      if (lane < 32) st64(ws, WS_S3 + mi * 256 + lane * 8, u32x2{nosent(cvtpk(v.x, v.y)), nosent(cvtpk(v.z, v.w))});
    }
    const u32 b = rid >> 4, nn = 32 * (rid & 15) + 4 * ((2 * quad + (idx >> 3)) ^ ((rid >> 1) & 1)) + trp, ns = nn >> 6, nl = nn & 63;
    const float xv = bflo(*(const GAS u16*)(px + b * 512 + nn));
    const float b2v = bflo(*(const GAS u16*)(pb2 + nn));
    const u32 shf = (nn & 1) * 16;
    wait_until(t0, T_RED);
    u32 gav, gbv;
    {
      const u32 so = WS_S3 + (nn >> 5) * 256 + b * 64 + (nn & 30) * 2;
      POLL_BEGIN
        gav = ld32(ws, so); gbv = ld32(ws, so + 12288);
      POLL_END(umax(gav, gbv) == SENT, __builtin_amdgcn_s_sleep(4))
    }
    float base, gbf = bflo(gbv >> shf);
    u32 tq = wait_heads_up(ws, x, lane, t0), dq = D_S6;
    {
      u32 pv8[8];
      const u32 so = WS_S5 + (x & 3) * 32768 + ns * 4096 + b * 128 + (nl & 62) * 2;
      POLL_BEGIN
        u32 mx = 0;
#pragma unroll
        for (int i = 0; i < 8; ++i) pv8[i] = ld32(ws, so + i * 512);
#pragma unroll
        for (int i = 0; i < 8; ++i) mx = umax(mx, pv8[i]);
      POLL_END(mx == SENT, dq = D_S6B; __builtin_amdgcn_s_sleep(1))
      if (dq == D_S6B) tq = now();
      float f[8];
#pragma unroll
      for (int i = 0; i < 8; ++i) f[i] = bflo(pv8[i] >> shf);
      const float at = ((f[0] + f[4]) + (f[2] + f[6])) + ((f[1] + f[5]) + (f[3] + f[7]));
      base = (xv + bflo(gav >> shf) * at) + gbf * b2v;
    }
    if (rid == 0 && tid == 0) __builtin_amdgcn_raw_buffer_store_b32(tag, ws, WS_EPOCH, 0, AUX_SC1);
    u32 so6 = WS_S6 + rid * 7168 + (lane >> 2) * 128 + trp * 16, so6l = lane < 32 ? so6 + 6144 : WS_BYTES;
    u32 oo = (lane & 4) ? 4096 : (b * 512 + nn) * 2;
    u32x4 sel = pair_selector(idx);
    const rsrc_t rout = mk_rsrc(pout, 4096);
    PIN("+v"(base), "+v"(gbf), "+v"(so6), "+v"(so6l), "+v"(oo), "+v"(sel));
    while (now() - tq < dq) {}
    {
      u32 spin = SPIN_CAP;
      asm volatile(
          ".Lask%=:\n\t"
          "buffer_load_dwordx4 v[128:131], %[so6], %[ws], 0 offen sc1\n\t"
          "buffer_load_dwordx4 v[132:135], %[so6], %[ws], 0 offen offset:2048 sc1\n\t"
          "buffer_load_dwordx4 v[136:139], %[so6], %[ws], %[c4k] offen sc1\n\t"
          "buffer_load_dwordx4 v[140:143], %[so6l], %[ws], 0 offen sc1\n\t"
          "s_waitcnt vmcnt(3)\n\t"
          MFMA "v[144:147], %[sel], v[128:131], 0\n\t"
          "v_max3_u32 v152, v128, v129, v130\n\t"
          "s_waitcnt vmcnt(2)\n\t"
          MFMA "v[144:147], %[sel], v[132:135], v[144:147]\n\t"
          "s_waitcnt vmcnt(1)\n\t"
          MFMA "v[144:147], %[sel], v[136:139], v[144:147]\n\t"
          "s_waitcnt vmcnt(0)\n\t"
          MFMA "v[144:147], %[sel], v[140:143], v[144:147]\n\t"
          "v_max3_u32 v152, v131, v132, v152\n\t"
          "v_max3_u32 v153, v133, v134, v135\n\t"
          "v_max3_u32 v152, v136, v137, v152\n\t"
          "v_max3_u32 v153, v138, v139, v153\n\t"
          "v_max3_u32 v152, v140, v141, v152\n\t"
          "v_max3_u32 v153, v142, v143, v153\n\t"
          "v_max_u32 v152, v152, v153\n\t"
          "v_cmp_eq_u32 vcc, -1, v152\n\t"
          "v_add_f32_dpp v148, v144, v144 row_ror:8 row_mask:0xf bank_mask:0x3\n\t"
          "v_add_f32_dpp v148, v145, v145 row_ror:8 row_mask:0xf bank_mask:0xc\n\t"
          "s_cbranch_vccz .Lgo%=\n\t"
          "s_sub_u32 %[spin], %[spin], 1\n\t"
          "s_cbranch_scc0 .Lask%=\n"
          ".Lgo%=:\n\t"
          "s_nop 0\n\t"
          "v_add_f32_dpp v148, v148, v148 row_shl:4 row_mask:0xf bank_mask:0xf bound_ctrl:1\n\t"
          "v_fma_f32 v148, %[gb], v148, %[base]\n\t"
          "v_cvt_pk_bf16_f32 v148, v148, v148\n\t"
          "buffer_store_short v148, %[oo], %[ro], 0 offen"
          : [spin] "+s"(spin)
          : [so6] "v"(so6), [so6l] "v"(so6l), [ws] "s"(ws), [c4k] "s"(4096u), [sel] "v"(sel), [gb] "v"(gbf), [base] "v"(base), [oo] "v"(oo), [ro] "s"(rout)
          : "memory", "vcc", "scc", TMP_CLOBBERS);
    }
    return;
  }

  {
    const u32 rb = r - 24, g = x;
    const rsrc_t ro = mk_rsrc(pwo, 4096u * 512 * 2);
    TAKE_PROLOGUE(8);
    float nv[4];
    {
      const float f[4] = {bflo(av0), bfhi(av0), bflo(av1), bfhi(av1)};
      float s1 = (f[0] + f[1]) + (f[2] + f[3]);
      s1 += dppf<XOR1>(s1); s1 += dppf<XOR2>(s1); s1 = rs16(s1, s1); s1 = rs32(s1, s1);
      if ((lane & 0x33) == 0) *(LAS float*)(L + L_ST + bA * 32 + w * 4) = s1;
      BARRIER();
      const f32x4 u = *(LAS f32x4*)(L + L_ST + bA * 32) + *(LAS f32x4*)(L + L_ST + bA * 32 + 16);
      const float mean = ((u.x + u.y) + (u.z + u.w)) * (1.f / 512.f);
      float s2 = 0.f;
#pragma unroll
      for (int e = 0; e < 4; ++e) s2 += (f[e] - mean) * (f[e] - mean);
      s2 += dppf<XOR1>(s2); s2 += dppf<XOR2>(s2); s2 = rs16(s2, s2); s2 = rs32(s2, s2);
      if ((lane & 0x33) == 0) *(LAS float*)(L + L_ST + 128 + bA * 32 + w * 4) = s2;
      BARRIER();
      const f32x4 v = *(LAS f32x4*)(L + L_ST + 128 + bA * 32) + *(LAS f32x4*)(L + L_ST + 128 + bA * 32 + 16);
      const float sd = rbf(__builtin_amdgcn_sqrtf(rbf(rbf(((v.x + v.y) + (v.z + v.w)) * (1.f / 512.f)) + 1e-5f))), m = rbf(mean);
      const float rs = __builtin_amdgcn_rcpf(sd);
#pragma unroll
      for (int e = 0; e < 4; ++e) {
        const float d = rbf(f[e] - m), q0 = d * rs;
        nv[e] = rbf(__builtin_fmaf(__builtin_fmaf(-q0, sd, d), rs, q0));
      }
    }
    u32 ta[4];
#pragma unroll
    for (int nt = 0; nt < 4; ++nt) ta[nt] = (4 * quad + trq) * 128 + ((nt ^ (2 * (quad & 1) + (trq >> 1))) << 5) + trp * 8;
    u32 so1 = WS_S1 + w * 1024 + lane * 16;
    u32 rw = L_RED + w * 1024 + quad * 256 + idx * 4, rr = L_RED + lane * 16, so2 = WS_S2 + (quad * 8 + g) * 2048 + rb * 256 + idx * 16;
    PIN("+v"(nv[0]), "+v"(nv[1]), "+v"(nv[2]), "+v"(nv[3]), "+v"(so1), "+v"(rw), "+v"(rr), "+v"(so2), "+v"(tag));
    WAIT_VM(0);
    {
      u32x4 wf[2][4];
#pragma unroll
      for (int t = 0; t < 2; ++t)
#pragma unroll
        for (int nt = 0; nt < 4; ++nt) {
          const u32 a = wl + (16 * quad + 8 * (trq & 1) + (trq >> 1)) * 128 + ((nt ^ ((trq & 1) | ((quad & 1) << 1))) << 5) + trp * 8 + t * 512;
          wf[t][nt] = tr_frag(L, a, a + 256);
        }
      SB();
      u32x4 gs, gc;
      POLL_BEGIN
        gs = ld128(ws, so1); gc = ld128(ws, so1 + 8192);
        const u32 bad = ((gs.y ^ tag) | (gs.w ^ tag)) | ((gc.y ^ tag) | (gc.w ^ tag));
      POLL_END(bad != 0, __builtin_amdgcn_s_sleep(1))
      const u32 c0 = cvtpk(1.f + bflo(gc.x), 1.f + bfhi(gc.x)), c1 = cvtpk(1.f + bflo(gc.z), 1.f + bfhi(gc.z));
      const u32 m0 = cvtpk(nv[0] * bflo(c0), nv[1] * bfhi(c0)), m1 = cvtpk(nv[2] * bflo(c1), nv[3] * bfhi(c1));
      const u32x4 ha = pair_frag(cvtpk(bflo(m0) + bflo(gs.x), bfhi(m0) + bfhi(gs.x)), cvtpk(bflo(m1) + bflo(gs.z), bfhi(m1) + bfhi(gs.z)));
#pragma unroll
      for (int nt = 0; nt < 4; ++nt) {
        const f32x4 d0 = mfma(ha, wf[0][nt], zero4), d1 = mfma(ha, wf[1][nt], zero4);
        *(LAS float*)(L + rw + nt * 64) = d0.x + d1.z;
      }
    }
    BARRIER();
    if (w == 0) {
      f32x4 u[8];
#pragma unroll
      for (int i = 0; i < 8; ++i) u[i] = *(LAS f32x4*)(L + rr + i * 1024);
      const f32x4 v = ((u[0] + u[1]) + (u[2] + u[3])) + ((u[4] + u[5]) + (u[6] + u[7]));
      st128(ws, so2, u32x4{cvtpk(v.x, v.y), tag, cvtpk(v.z, v.w), tag});
    }
    SB();
    if (w == 0) wait_flags(ws, lane, tag);
    const u32 go = (x + 1) & 7;
    u32 so4 = WS_S4 + (go * 16 + quad) * S4_SLOT + w * 256 + ((idx ^ quad) << 4);
    u32 so5 = WS_S5 + (rb * 8 + go) * 512 + lane * 8 + (w & 3) * 32768;
    u32 wa_[4];
#pragma unroll
    for (int t = 0; t < 4; ++t) wa_[t] = 2 * wl + ta[t] + 8192;
    u32 som = WS_S4 + (go * 16 + quad * 4 + trp) * S4_SLOT + 1024 + w * 8;
    u32 ord = lbase + wl + trq * 1088 + trp * 256 + ((quad ^ trp) << 4);
    rw += lbase; rr += lbase;
    u32 vo = (16 * ((lane >> 3) & 3) + (lane >> 5)) * 1024 + 16 * ((lane & 7) ^ (quad << 1));
    PIN("+v"(so4), "+v"(so5), "+v"(som), "+v"(ord), "+v"(rw), "+v"(rr));
    PIN("+v"(wa_[0]), "+v"(wa_[1]), "+v"(wa_[2]), "+v"(wa_[3]), "+v"(vo));
    if (w == 0) wait_until(t0, rb == 3 ? T_WO - D_SLOW : T_WO);
    __builtin_amdgcn_s_barrier();
#pragma unroll
    for (int i = 0; i < 8; ++i)
      dma<0>(ro, vo, (go * 512 + (w >> 1) * 128 + 8 * (w & 1) + 4 * (i >> 2) + 2 * (i & 1) + 64 * ((i >> 1) & 1)) * 1024 + rb * 128,
                 lbase + wl + 8192 + i * 1024);
#define O_ASK \
        "s_mov_b32 m0, %[m0]\n\ts_nop 0\n\t" \
        "buffer_load_dwordx4 %[so4], %[ws], 0 offen sc1 lds\n\ts_addk_i32 m0, 0x440\n\ts_nop 0\n\t" \
        "buffer_load_dwordx4 %[so4], %[ws], %[c1] offen sc1 lds\n\ts_addk_i32 m0, 0x440\n\ts_nop 0\n\t" \
        "buffer_load_dwordx4 %[so4], %[ws], %[c2] offen sc1 lds\n\ts_addk_i32 m0, 0x440\n\ts_nop 0\n\t" \
        "buffer_load_dwordx4 %[so4], %[ws], %[c3] offen sc1 lds\n\t"
#define O_ASK_OPS [m0] "s"(lbase + wl), [so4] "v"(so4), [ws] "s"(ws), [c1] "s"(4 * S4_SLOT), [c2] "s"(8 * S4_SLOT), [c3] "s"(12 * S4_SLOT)
#define O_ML_ASK() asm volatile("buffer_load_dwordx2 %[ml], %[som], %[ws], 0 offen sc1\n\t" O_ASK : [ml] "=&v"(ml0) : O_ASK_OPS, [som] "v"(som) : "memory", "scc")
#define O_ML_TAKE() asm volatile("s_waitcnt vmcnt(4)\n\tv_mov_b32 %0, %2\n\tv_mov_b32 %1, %3" : "=&v"(ml.x), "=&v"(ml.y) : "v"(ml0.x), "v"(ml0.y) : "memory")
    WAIT_VM(0);
    u32x2 ml, ml0;
    if (w >= 4) {
      __builtin_amdgcn_s_barrier();
      if (w == 4 && rb == 3) {
        POLL_BEGIN
          const u32x2 t = ld64(ws, WS_S4 + (go * 16 + idx) * S4_SLOT + 1024 + quad * 8);
        POLL_END(umax(t.x, t.y) == SENT, (void)0)
        __builtin_amdgcn_raw_buffer_store_b32(nosent(now()), ws, lane < 8 ? WS_HU + lane * 1024 + go * 128 : WS_BYTES, 0, AUX_SC1);
      }
      return;
    }
    __builtin_amdgcn_s_barrier();
    OPAQUE();
    u32x4 wf[4][4];
    O_ML_ASK();
#pragma unroll
    for (int t = 0; t < 4; ++t)
#pragma unroll
      for (int nt = 0; nt < 4; ++nt) { const u32 a = wa_[nt] + (t >> 1) * L_WAVE + (t & 1) * 4096; wf[t][nt] = tr_frag(L, a, a + 2048); }
    SB();
    O_ML_TAKE();
    if (!none(umax(ml.x, ml.y) == SENT)) {
      POLL_BEGIN
        ml = ld64(ws, som);
      POLL_END(umax(ml.x, ml.y) == SENT, (void)0)
      asm volatile(O_ASK ::O_ASK_OPS : "memory", "scc");
    }
    float w0, w1, w2, w3;
    {
      const float m = __uint_as_float(ml.x);
      float M = vmax(m, dppf<XOR1>(m)); M = vmax(M, dppf<XOR2>(M));
      const float e = __builtin_amdgcn_exp2f((m - M) * LOG2E);
      float Ls = e * __uint_as_float(ml.y);
      Ls += dppf<XOR1>(Ls); Ls += dppf<XOR2>(Ls);
      const float wv = e * __builtin_amdgcn_rcpf(Ls);
      w0 = dppf<QP(0, 0, 0, 0)>(wv); w1 = dppf<QP(1, 1, 1, 1)>(wv); w2 = dppf<QP(2, 2, 2, 2)>(wv); w3 = dppf<QP(3, 3, 3, 3)>(wv);
    }
    {
      u32 spin = SPIN_CAP;
      asm volatile(
          "s_branch .Ltake%=\n"
          ".Lask%=:\n\t"
          O_ASK
          ".Ltake%=:\n\t"
          "s_waitcnt vmcnt(0)\n\t"
          "ds_read_b128 v[128:131], %[ord]\n\t"
          "ds_read_b128 v[132:135], %[ord] offset:64\n\t"
          "ds_read_b128 v[136:139], %[ord] offset:128\n\t"
          "ds_read_b128 v[140:143], %[ord] offset:192\n\t"
          "s_waitcnt lgkmcnt(3)\n\t"
          MFMA "v[144:147], v[128:131], %[f00], 0\n\t"
          "v_max3_u32 v160, v128, v129, v130\n\t"
          "s_waitcnt lgkmcnt(2)\n\t"
          MFMA "v[148:151], v[128:131], %[f01], 0\n\t"
          "v_max3_u32 v160, v131, v132, v160\n\t"
          "v_max3_u32 v161, v133, v134, v135\n\t"
          MFMA "v[144:147], v[132:135], %[f10], v[144:147]\n\t"
          "s_waitcnt lgkmcnt(0)\n\t"
          "v_max3_u32 v160, v136, v137, v160\n\t"
          MFMA "v[148:151], v[132:135], %[f11], v[148:151]\n\t"
          "v_max3_u32 v161, v138, v139, v161\n\t"
          "v_max3_u32 v160, v140, v141, v160\n\t"
          MFMA "v[144:147], v[136:139], %[f20], v[144:147]\n\t"
          "v_max3_u32 v161, v142, v143, v161\n\t"
          "v_max_u32 v160, v160, v161\n\t"
          MFMA "v[148:151], v[136:139], %[f21], v[148:151]\n\t"
          "v_cmp_eq_u32 vcc, -1, v160\n\t"
          MFMA "v[144:147], v[140:143], %[f30], v[144:147]\n\t"
          "s_cbranch_vccz .Lgo%=\n\t"
          "s_sub_u32 %[spin], %[spin], 1\n\t"
          "s_cbranch_scc0 .Lask%=\n"
          ".Lgo%=:\n\t"
          MFMA "v[148:151], v[140:143], %[f31], v[148:151]\n\t"
          MFMA "v[152:155], v[128:131], %[f02], 0\n\t"
          MFMA "v[156:159], v[128:131], %[f03], 0\n\t"
          MFMA "v[152:155], v[132:135], %[f12], v[152:155]\n\t"
          "s_nop 1\n\t"
          MFMA "v[152:155], v[136:139], %[f22], v[152:155]\n\t"
          "v_mul_f32 v160, %[w0], v144\n\t"
          "v_fmac_f32 v160, %[w1], v145\n\t"
          MFMA "v[152:155], v[140:143], %[f32], v[152:155]\n\t"
          "v_mul_f32 v161, %[w0], v148\n\t"
          "v_fmac_f32 v160, %[w2], v146\n\t"
          MFMA "v[156:159], v[132:135], %[f13], v[156:159]\n\t"
          "v_fmac_f32 v161, %[w1], v149\n\t"
          "v_fmac_f32 v160, %[w3], v147\n\t"
          MFMA "v[156:159], v[136:139], %[f23], v[156:159]\n\t"
          "v_fmac_f32 v161, %[w2], v150\n\t"
          "v_fmac_f32 v161, %[w3], v151\n\t"
          MFMA "v[156:159], v[140:143], %[f33], v[156:159]\n\t"
          "v_mul_f32 v162, %[w0], v152\n\t"
          "ds_write2_b32 %[rw], v160, v161 offset1:16\n\t"
          "v_fmac_f32 v162, %[w1], v153\n\t"
          "v_fmac_f32 v162, %[w2], v154\n\t"
          "v_fmac_f32 v162, %[w3], v155\n\t"
          "s_nop 2\n\t"
          "v_mul_f32 v163, %[w0], v156\n\t"
          "v_fmac_f32 v163, %[w1], v157\n\t"
          "v_fmac_f32 v163, %[w2], v158\n\t"
          "v_fmac_f32 v163, %[w3], v159\n\t"
          "ds_write2_b32 %[rw], v162, v163 offset0:32 offset1:48\n\t"
          "s_waitcnt lgkmcnt(0)\n\t"
          "s_barrier"
          : [spin] "+s"(spin)
          : O_ASK_OPS, [ord] "v"(ord), [rw] "v"(rw), [w0] "v"(w0), [w1] "v"(w1), [w2] "v"(w2), [w3] "v"(w3),
            [f00] "v"(wf[0][0]), [f01] "v"(wf[0][1]), [f02] "v"(wf[0][2]), [f03] "v"(wf[0][3]), [f10] "v"(wf[1][0]), [f11] "v"(wf[1][1]),
            [f12] "v"(wf[1][2]), [f13] "v"(wf[1][3]), [f20] "v"(wf[2][0]), [f21] "v"(wf[2][1]), [f22] "v"(wf[2][2]), [f23] "v"(wf[2][3]),
            [f30] "v"(wf[3][0]), [f31] "v"(wf[3][1]), [f32] "v"(wf[3][2]), [f33] "v"(wf[3][3])
          : "memory", "vcc", "scc", TMP_CLOBBERS);
      asm volatile(
          "ds_read_b128 v[128:131], %[rr]\n\t"
          "ds_read_b128 v[132:135], %[rr] offset:1024\n\t"
          "ds_read_b128 v[136:139], %[rr] offset:2048\n\t"
          "ds_read_b128 v[140:143], %[rr] offset:3072\n\t"
          "s_waitcnt lgkmcnt(2)\n\t"
          "v_pk_add_f32 v[128:129], v[128:129], v[132:133]\n\t"
          "v_pk_add_f32 v[130:131], v[130:131], v[134:135]\n\t"
          "s_waitcnt lgkmcnt(0)\n\t"
          "v_pk_add_f32 v[136:137], v[136:137], v[140:141]\n\t"
          "v_pk_add_f32 v[138:139], v[138:139], v[142:143]\n\t"
          "v_pk_add_f32 v[128:129], v[128:129], v[136:137]\n\t"
          "v_pk_add_f32 v[130:131], v[130:131], v[138:139]\n\t"
          "v_cvt_pk_bf16_f32 v132, v128, v129\n\t"
          "v_cvt_pk_bf16_f32 v133, v130, v131\n\t"
          "v_min_u32 v132, -2, v132\n\t"
          "v_min_u32 v133, -2, v133\n\t"
          "buffer_store_dwordx2 v[132:133], %[so5], %[ws], 0 offen sc1"
          :
          : [rr] "v"(rr), [so5] "v"(so5), [ws] "s"(ws)
          : "memory", TMP_CLOBBERS);
    }
  }
}

extern "C" void kernel_launch(void* const* d_in, const int* in_sizes, int n_in, void* d_out, int out_size, void* d_ws,
                              size_t ws_size, hipStream_t stream) {
  fused_decode<<<256, 512, 0, stream>>>((const u16*)d_in[1], 4u * 8 * 8192 * 128 * 2, 0x00020000u, (const u16*)d_in[11], 512u * 3072 * 2,
                                        0x00020000u, (const u16*)d_in[3], 512u * 4096 * 2, 0x00020000u, (const u16*)d_in[10],
                                        (const u16*)d_in[0], (const u16*)d_in[2], (const u16*)d_in[4], (const u16*)d_in[6],
                                        (const u16*)d_in[7], (const u16*)d_in[8], (const u16*)d_in[9], (u16*)d_out,
                                        (unsigned char*)d_ws);
}
```
